```python
import torch

def _softmax(x, axis=-1):
    return torch.softmax(x, dim=axis)
def _gelu(x):
    return torch.nn.functional.gelu(x, approximate="tanh")
def _silu(x):
    return torch.nn.functional.silu(x)
def _sigmoid(x):
    return torch.sigmoid(x)
def _layernorm(x, axis=-1, eps=1e-5):
    m = x.mean(dim=axis, keepdim=True)
    v = x.var(dim=axis, keepdim=True, unbiased=False)
    return (x - m) / torch.sqrt(v + eps)
def _scatter_add(tgt, idx, src, axis=0):
    return tgt.clone().index_add_(axis, idx, src)
def _segment_sum(src, seg, n):
    out = torch.zeros((n,) + src.shape[1:], dtype=src.dtype)
    return out.index_add_(0, seg, src)
def _pad(x, pw):
    flat = tuple(v for pair in reversed(pw) for v in pair)
    return torch.nn.functional.pad(x, flat)

kernel_name = 'standalone__graph_attention_gat__graph'

def setup_inputs():

    _w = (lambda a: (lambda t: t.to(torch.bfloat16) if t.is_floating_point() else t)(torch.as_tensor(a)))
    N, deg, D = 10000, 16, 256
    H = 4
    E = N * deg
    return {
        "x": _w((lambda *s: torch.randn(*s, dtype=torch.bfloat16) * 0.4)(N, D) * 0.1),
        "src": _w((lambda lo, hi, *s: torch.randint(lo, hi, s))(0, N, E).to(torch.int64)),
        "dst": _w((lambda lo, hi, *s: torch.randint(lo, hi, s))(0, N, E).to(torch.int64)),
        "w": _w((lambda *s: torch.randn(*s, dtype=torch.bfloat16) * 1.6)(H, D, D) * (1.0 / D ** 0.5)),
        "a_src": _w((lambda *s: torch.randn(*s, dtype=torch.bfloat16) * 1.6)(H, D) * 0.1),
        "a_dst": _w((lambda *s: torch.randn(*s, dtype=torch.bfloat16) * 1.6)(H, D) * 0.1),
    }

def reference(x, src, dst, w, a_src, a_dst):
    N, D = x.shape
    H = w.shape[0]
    h = torch.einsum('nd,hdf->nhf', x, w)
    hs = h[src]
    hd = h[dst]
    e = torch.einsum('ehf,hf->eh', hs, a_src) + torch.einsum('ehf,hf->eh', hd, a_dst)
    e = torch.where(e > 0, e, 0.2 * e)
    exp_e = torch.exp(e)
    denom = _segment_sum(exp_e, dst, N) + 1e-9
    alpha = exp_e / denom[dst]
    out = _segment_sum(alpha[:, :, None] * hs, dst, N)
    return out.mean(1)
```

```cpp
#include <hip/hip_runtime.h>
#include <hip/hip_bf16.h>
#include <stdint.h>

typedef __attribute__((ext_vector_type(8))) __bf16 bf16x8;
typedef __attribute__((ext_vector_type(2))) __bf16 bf16x2;
typedef __attribute__((ext_vector_type(4))) float f32x4;
typedef __attribute__((ext_vector_type(2))) float f32x2;
typedef __attribute__((ext_vector_type(4))) unsigned u32x4;
typedef __attribute__((ext_vector_type(4))) int i32x4;

constexpr int NN = 10000, NE = 160000, DD = 256, HH = 4;
constexpr int T = 40, NT = NN / T;
constexpr int NB = 64, CAP = 32, EPB = NE / NB;
constexpr int SB = 160;
constexpr unsigned EMPTY = 0xFFFFFFFFu, OVF = 0xFFFFFFFEu;
constexpr int NG = T / 4;
constexpr int GCAP = 128;

constexpr size_t WS_CELLS = 0;
constexpr size_t WS_WT    = 2097152;
constexpr size_t WS_SSRC  = WS_WT + 524288;
constexpr size_t WS_SDST  = WS_SSRC + 655360;

constexpr int K1_BIN = NB;
constexpr int K1_TR  = 16;
constexpr int K1_S   = 256;
constexpr int K1_GRID = K1_BIN + K1_TR + K1_S;

__device__ __forceinline__ unsigned pack_bf16(float a, float b) {
    f32x2 v = {a, b};
    bf16x2 r = __builtin_convertvector(v, bf16x2);
    return __builtin_bit_cast(unsigned, r);
}
__device__ __forceinline__ float bf16_round(float a) {
    __bf16 r = (__bf16)a;
    return (float)r;
}
__device__ __forceinline__ f32x4 mfma16(u32x4 a, u32x4 b, f32x4 c) {
    return __builtin_amdgcn_mfma_f32_16x16x32_bf16(__builtin_bit_cast(bf16x8, a), __builtin_bit_cast(bf16x8, b), c, 0, 0, 0);
}
__device__ __forceinline__ unsigned perm_lo(unsigned a, unsigned b) { return __builtin_amdgcn_perm(b, a, 0x05040100u); }
__device__ __forceinline__ unsigned perm_hi(unsigned a, unsigned b) { return __builtin_amdgcn_perm(b, a, 0x07060302u); }

template <int CTRL>
__device__ __forceinline__ float dpp_f(float v) {
    return __builtin_bit_cast(float, __builtin_amdgcn_update_dpp(0, __builtin_bit_cast(int, v), CTRL, 0xF, 0xF, true));
}
__device__ __forceinline__ float quad_transpose_sum(f32x4 v, int j) {
    const bool b0 = j & 1, b1 = j & 2;
    float t0 = b0 ? v[1] : v[0], s0 = b0 ? v[0] : v[1];
    float t1 = b0 ? v[3] : v[2], s1 = b0 ? v[2] : v[3];
    t0 += dpp_f<0xB1>(s0);
    t1 += dpp_f<0xB1>(s1);
    float keep = b1 ? t1 : t0, send = b1 ? t0 : t1;
    return keep + dpp_f<0x4E>(send);
}

__global__ __launch_bounds__(256) void gat_k1(const unsigned short* __restrict__ x, const int* __restrict__ src,
                                              const int* __restrict__ dst, const unsigned short* __restrict__ w,
                                              const unsigned short* __restrict__ a_src,
                                              const unsigned short* __restrict__ a_dst, char* __restrict__ ws) {
    __shared__ __attribute__((aligned(16))) unsigned smem[NT * CAP + 256];
    const int tid = threadIdx.x, lane = tid & 63, wv = tid >> 6;
    const int b = blockIdx.x;
    if (b < K1_BIN) {
        unsigned* stage = smem;
        int* cnt = (int*)(smem + NT * CAP);
        const i32x4* d4 = (const i32x4*)(dst + b * EPB);
        const i32x4* s4 = (const i32x4*)(src + b * EPB);
        i32x4 dv[3], sv[3];
#pragma unroll
        for (int r = 0; r < 3; ++r) {
            const int i = tid + 256 * r;
            if (i < EPB / 4) { dv[r] = d4[i]; sv[r] = s4[i]; }
        }
        const u32x4 e4 = {EMPTY, EMPTY, EMPTY, EMPTY};
        for (int i = tid; i < NT * CAP / 4; i += 256) ((u32x4*)stage)[i] = e4;
        cnt[tid] = 0;
        __syncthreads();
#pragma unroll
        for (int r = 0; r < 3; ++r) {
            const int i = tid + 256 * r;
            if (i < EPB / 4) {
#pragma unroll
                for (int k = 0; k < 4; ++k) {
                    const unsigned d = (unsigned)dv[r][k], s = (unsigned)sv[r][k];
                    const unsigned t = d / (unsigned)T;
                    const int pos = atomicAdd(&cnt[t], 1);
                    if (pos < CAP) stage[t * CAP + pos] = s | ((d - t * T) << 16);
                }
            }
        }
        __syncthreads();
        u32x4* cells4 = (u32x4*)(ws + WS_CELLS);
        for (int i = tid; i < NT * CAP / 4; i += 256) {
            const int t = i >> 3, piece = i & 7;
            u32x4 v = ((u32x4*)stage)[i];
            if (piece == 0 && cnt[t] > CAP) v[0] = OVF;
            cells4[(t * NB + b) * 8 + piece] = v;
        }
    } else if (b < K1_BIN + K1_TR) {
        const int ks = (b - K1_BIN) * 2 + (tid >> 7);
        const int kc = (tid >> 5) & 3, fc = tid & 31;
        u32x4 r[8];
#pragma unroll
        for (int j = 0; j < 8; ++j) r[j] = *(const u32x4*)(w + (size_t)(32 * ks + 8 * kc + j) * DD + 8 * fc);
        const int wq = fc >> 2, gp = fc & 3;
        char* wt = ws + WS_WT;
#pragma unroll
        for (int i = 0; i < 8; ++i) {
            u32x4 o;
#pragma unroll
            for (int p = 0; p < 4; ++p)
                o[p] = (i & 1) ? perm_hi(r[2 * p][i >> 1], r[2 * p + 1][i >> 1]) : perm_lo(r[2 * p][i >> 1], r[2 * p + 1][i >> 1]);
            const int t = i >> 2, reg = i & 3;
            *(u32x4*)(wt + ((size_t)((wq * 32 + ks) * 2 + t) * 1024) + (kc * 16 + 4 * gp + reg) * 16) = o;
        }
    } else {
        const int sb = b - K1_BIN - K1_TR;
        const int q = sb >> 6, ib = sb & 63;
        const int n0 = ib * SB;
        if (n0 >= NN) return;
        float* wa = (float*)smem;
        const int m = lane & 15, kc = lane >> 4;
        u32x4 xb[3][2];
#pragma unroll
        for (int r = 0; r < 3; ++r) {
            const int nt = wv + 4 * r;
            int node = n0 + nt * 16 + m;
            node = node < NN ? node : NN - 1;
            if (nt < SB / 16) {
#pragma unroll
                for (int k2 = 0; k2 < 2; ++k2)
                    xb[r][k2] = *(const u32x4*)(x + (size_t)node * DD + 64 * q + 32 * k2 + 8 * kc);
            }
        }
        const int h = wv;
        u32x4 ab[8];
#pragma unroll
        for (int ks = 0; ks < 8; ++ks) {
            u32x4 v = {0u, 0u, 0u, 0u};
            if (m < 2) v = *(const u32x4*)((m == 0 ? a_src : a_dst) + h * DD + 32 * ks + 8 * kc);
            ab[ks] = v;
        }
#pragma unroll
        for (int mt = 0; mt < 4; ++mt) {
            f32x4 acc = {0.f, 0.f, 0.f, 0.f};
            const unsigned short* wrow = w + ((size_t)h * DD + 64 * q + 16 * mt + m) * DD + 8 * kc;
#pragma unroll
            for (int ks = 0; ks < 8; ++ks) {
                const u32x4 af = *(const u32x4*)(wrow + 32 * ks);
                acc = mfma16(af, ab[ks], acc);
            }
            if (m < 2) *(f32x4*)(wa + (m * 4 + h) * 64 + 16 * mt + 4 * kc) = acc;
        }
        __syncthreads();
        u32x4 ah[2][3];
#pragma unroll
        for (int k2 = 0; k2 < 2; ++k2) {
            float v[8];
            const f32x4 v0 = *(const f32x4*)(wa + (m & 7) * 64 + 32 * k2 + 8 * kc);
            const f32x4 v1 = *(const f32x4*)(wa + (m & 7) * 64 + 32 * k2 + 8 * kc + 4);
#pragma unroll
            for (int j = 0; j < 4; ++j) { v[j] = m < 8 ? v0[j] : 0.f; v[4 + j] = m < 8 ? v1[j] : 0.f; }
#pragma unroll
            for (int s = 0; s < 3; ++s) {
#pragma unroll
                for (int p = 0; p < 4; ++p) {
                    const float h0 = bf16_round(v[2 * p]), h1 = bf16_round(v[2 * p + 1]);
                    ah[k2][s][p] = pack_bf16(h0, h1);
                    v[2 * p] -= h0; v[2 * p + 1] -= h1;
                }
            }
        }
        float* ssrc = (float*)(ws + WS_SSRC);
        float* sdst = (float*)(ws + WS_SDST);
#pragma unroll
        for (int r = 0; r < 3; ++r) {
            const int nt = wv + 4 * r;
            if (nt < SB / 16) {
                f32x4 acc = {0.f, 0.f, 0.f, 0.f};
#pragma unroll
                for (int s = 2; s >= 0; --s)
#pragma unroll
                    for (int k2 = 0; k2 < 2; ++k2) acc = mfma16(ah[k2][s], xb[r][k2], acc);
                const int node = n0 + nt * 16 + m;
                if (kc < 2 && node < NN) *(f32x4*)((kc == 0 ? ssrc : sdst) + (size_t)node * 16 + q * 4) = acc;
            }
        }
    }
}

constexpr int L_Z = 0;
constexpr int L_P = L_Z + T * 2048;
constexpr int L_EL = L_P + NG * 4096;
constexpr int L_FL = L_EL + NG * GCAP * 4;
constexpr int L_SD = L_FL + NG * GCAP * 4;
constexpr int L_CT = L_SD + T * 16;
constexpr int L_TOTAL = L_CT + 64;
constexpr unsigned OOB = 0x80000000u;

__device__ __forceinline__ int zaddr(int node, int ch) {
    return node * 2048 + 16 * (ch ^ ((node ^ (2 * (ch >> 5))) & 15));
}

__global__ __launch_bounds__(512, 2) void gat_k2(const unsigned short* __restrict__ x, const int* __restrict__ src,
                                                 const int* __restrict__ dst, const char* __restrict__ ws,
                                                 unsigned short* __restrict__ out) {
    extern __shared__ __attribute__((aligned(16))) char smem[];
    const int tid = threadIdx.x, lane = tid & 63;
    const int wv = __builtin_amdgcn_readfirstlane(tid >> 6);
    const int tile = blockIdx.x;
    const int n0 = tile * T;
    unsigned* elist = (unsigned*)(smem + L_EL);
    unsigned* flat = (unsigned*)(smem + L_FL);
    float* sd = (float*)(smem + L_SD);
    int* cts = (int*)(smem + L_CT);
    const float* ssrc = (const float*)(ws + WS_SSRC);
    const float* sdst = (const float*)(ws + WS_SDST);

    const u32x4 ce = ((const u32x4*)(ws + WS_CELLS))[tile * (NB * CAP / 4) + tid];
    f32x4 sdv = {0.f, 0.f, 0.f, 0.f};
    if (tid < T * 4) sdv = *(const f32x4*)(sdst + (size_t)n0 * 16 + tid * 4);
    {
        const u32x4 z4 = {0u, 0u, 0u, 0u};
#pragma unroll
        for (int i = 0; i < NG * 4096 / 16 / 512; ++i) ((u32x4*)(smem + L_P))[tid + 512 * i] = z4;
        const u32x4 o4 = {OOB, OOB, OOB, OOB};
        if (tid < NG * GCAP / 4) ((u32x4*)elist)[tid] = o4;
        if (tid < 16) cts[tid] = 0;
    }
    if (tid < 192) {
        const float s = quad_transpose_sum(sdv, tid & 3);
        if (tid < T * 4) sd[tid] = s;
    }
    __syncthreads();
#pragma unroll
    for (int k = 0; k < 4; ++k) {
        const unsigned e = ce[k];
        if (e == OVF) cts[11] = 1;
        else if (e != EMPTY) {
            const unsigned s = e & 0xFFFFu, dl = e >> 16, grp = dl >> 2;
            const int pos = atomicAdd(&cts[grp], 1);
            if (pos < GCAP) {
                elist[grp * GCAP + pos] = s * 512u;
                const int fi = atomicAdd(&cts[10], 1);
                flat[fi] = s | (dl << 14) | ((unsigned)pos << 20);
            } else cts[11] = 1;
        }
    }
    __syncthreads();
    const int slow = __builtin_amdgcn_readfirstlane(cts[11]);
    const int c16 = lane & 15, g = lane >> 4;

    if (!slow) {
        const int ntask = __builtin_amdgcn_readfirstlane(cts[10]) * 4;
        const int j = tid & 3;
        for (int base = 0; base < ntask; base += 512 * 6) {
            unsigned fe[6]; f32x4 sv[6];
#pragma unroll
            for (int r = 0; r < 6; ++r) {
                const int task = base + tid + 512 * r;
                fe[r] = 0; sv[r] = f32x4{0.f, 0.f, 0.f, 0.f};
                if (task < ntask) {
                    fe[r] = flat[task >> 2];
                    sv[r] = *(const f32x4*)(ssrc + (size_t)(fe[r] & 0x3FFFu) * 16 + j * 4);
                }
            }
#pragma unroll
            for (int r = 0; r < 6; ++r) {
                const int task = base + tid + 512 * r;
                const float ss = quad_transpose_sum(sv[r], j);
                if (task < ntask) {
                    const unsigned dl = (fe[r] >> 14) & 63u, pos = fe[r] >> 20;
                    float e = ss + sd[dl * 4 + j];
                    e = e > 0.f ? e : 0.2f * e;
                    const float p = __expf(e);
                    const __bf16 pb = (__bf16)p;
                    const unsigned grp = dl >> 2, nl = dl & 3;
                    const unsigned k = pos & 31;
                    *(__bf16*)(smem + L_P + grp * 4096 + (pos >> 5) * 1024 + ((k >> 3) * 16 + nl * 4 + j) * 16 + (k & 7) * 2) = pb;
                }
            }
        }
        __syncthreads();
        const __amdgpu_buffer_rsrc_t xr = __builtin_amdgcn_make_buffer_rsrc((void*)x, 0, NN * DD * 2, 0x00020000);
        const u32x4 ones = {0x3F803F80u, 0x3F803F80u, 0x3F803F80u, 0x3F803F80u};
        for (int u = wv; u < 2 * NG; u += 8) {
            const int half = u >= NG ? 1 : 0, grp = u - half * NG;
            const int nks = (__builtin_amdgcn_readfirstlane(cts[grp]) + 31) >> 5;
            f32x4 acc[8], accd = {0.f, 0.f, 0.f, 0.f};
#pragma unroll
            for (int i = 0; i < 8; ++i) acc[i] = f32x4{0.f, 0.f, 0.f, 0.f};
            const unsigned lo = c16 * 16 + half * 256;
            u32x4 r[3][8];
#pragma unroll
            for (int ks = 0; ks < 3; ++ks) {
                const u32x4 o0 = *(const u32x4*)(elist + grp * GCAP + ks * 32 + 8 * g);
                const u32x4 o1 = *(const u32x4*)(elist + grp * GCAP + ks * 32 + 8 * g + 4);
#pragma unroll
                for (int jj = 0; jj < 8; ++jj)
                    r[ks][jj] = __builtin_amdgcn_raw_buffer_load_b128(xr, (jj < 4 ? o0[jj] : o1[jj - 4]) + lo, 0, 0);
            }
#pragma unroll
            for (int ks = 0; ks < 3; ++ks) {
                const u32x4 pf = *(const u32x4*)(smem + L_P + grp * 4096 + ks * 1024 + lane * 16);
                accd = mfma16(ones, pf, accd);
#pragma unroll
                for (int i = 0; i < 8; ++i) {
                    u32x4 f;
#pragma unroll
                    for (int p = 0; p < 4; ++p)
                        f[p] = (i & 1) ? perm_hi(r[ks][2 * p][i >> 1], r[ks][2 * p + 1][i >> 1])
                                       : perm_lo(r[ks][2 * p][i >> 1], r[ks][2 * p + 1][i >> 1]);
                    acc[i] = mfma16(f, pf, acc[i]);
                }
            }
            if (nks > 3) {
                const int ks = 3;
                const u32x4 o0 = *(const u32x4*)(elist + grp * GCAP + ks * 32 + 8 * g);
                const u32x4 o1 = *(const u32x4*)(elist + grp * GCAP + ks * 32 + 8 * g + 4);
                u32x4 q[8];
#pragma unroll
                for (int jj = 0; jj < 8; ++jj)
                    q[jj] = __builtin_amdgcn_raw_buffer_load_b128(xr, (jj < 4 ? o0[jj] : o1[jj - 4]) + lo, 0, 0);
                const u32x4 pf = *(const u32x4*)(smem + L_P + grp * 4096 + ks * 1024 + lane * 16);
                accd = mfma16(ones, pf, accd);
#pragma unroll
                for (int i = 0; i < 8; ++i) {
                    u32x4 f;
#pragma unroll
                    for (int p = 0; p < 4; ++p)
                        f[p] = (i & 1) ? perm_hi(q[2 * p][i >> 1], q[2 * p + 1][i >> 1])
                                       : perm_lo(q[2 * p][i >> 1], q[2 * p + 1][i >> 1]);
                    acc[i] = mfma16(f, pf, acc[i]);
                }
            }
            const float sc = 0.25f / (accd[0] + 1e-9f);
            const int node = grp * 4 + (c16 >> 2), h = c16 & 3;
#pragma unroll
            for (int reg = 0; reg < 4; ++reg) {
                u32x4 o;
#pragma unroll
                for (int p = 0; p < 4; ++p) o[p] = pack_bf16(acc[2 * p][reg] * sc, acc[2 * p + 1][reg] * sc);
                *(u32x4*)(smem + L_Z + zaddr(node, h * 32 + half * 16 + 4 * g + reg)) = o;
            }
        }
    } else {
        for (int nl = wv; nl < T; nl += 8) {
            const int node = n0 + nl;
            float acc[4][4], den[4];
#pragma unroll
            for (int h = 0; h < 4; ++h) { den[h] = 0.f;
#pragma unroll
                for (int i = 0; i < 4; ++i) acc[h][i] = 0.f; }
            for (int e0 = 0; e0 < NE; e0 += 64) {
                const int e = e0 + lane;
                const int dv = dst[e];
                const int sv = src[e];
                unsigned long long mask = __ballot(dv == node);
                while (mask) {
                    const int bit = __builtin_ctzll(mask);
                    mask &= mask - 1;
                    const int s = __shfl(sv, bit, 64);
                    const f32x4 q0 = *(const f32x4*)(ssrc + (size_t)s * 16);
                    const f32x4 q1 = *(const f32x4*)(ssrc + (size_t)s * 16 + 4);
                    const f32x4 q2 = *(const f32x4*)(ssrc + (size_t)s * 16 + 8);
                    const f32x4 q3 = *(const f32x4*)(ssrc + (size_t)s * 16 + 12);
                    const uint2 xv = *(const uint2*)(x + (size_t)s * DD + lane * 4);
                    const float xf[4] = {__uint_as_float(xv.x << 16), __uint_as_float(xv.x & 0xFFFF0000u),
                                         __uint_as_float(xv.y << 16), __uint_as_float(xv.y & 0xFFFF0000u)};
#pragma unroll
                    for (int h = 0; h < 4; ++h) {
                        float ev = ((q0[h] + q1[h]) + (q2[h] + q3[h])) + sd[nl * 4 + h];
                        ev = ev > 0.f ? ev : 0.2f * ev;
                        const float p = bf16_round(__expf(ev));
                        den[h] += p;
#pragma unroll
                        for (int i = 0; i < 4; ++i) acc[h][i] += p * xf[i];
                    }
                }
            }
#pragma unroll
            for (int h = 0; h < 4; ++h) {
                const float sc = 0.25f / (den[h] + 1e-9f);
                uint2 o;
                o.x = pack_bf16(acc[h][0] * sc, acc[h][1] * sc);
                o.y = pack_bf16(acc[h][2] * sc, acc[h][3] * sc);
                *(uint2*)(smem + L_Z + zaddr(nl, h * 32 + (lane >> 1)) + (lane & 1) * 8) = o;
            }
        }
    }
    __syncthreads();
    {
        f32x4 acc[2][3];
#pragma unroll
        for (int t = 0; t < 2; ++t)
#pragma unroll
            for (int nt = 0; nt < 3; ++nt) acc[t][nt] = f32x4{0.f, 0.f, 0.f, 0.f};
        const u32x4* wt = (const u32x4*)(ws + WS_WT) + (size_t)wv * 64 * 64 + lane;
#pragma unroll 8
        for (int ks = 0; ks < 32; ++ks) {
            const u32x4 w0 = wt[(ks * 2 + 0) * 64];
            const u32x4 w1 = wt[(ks * 2 + 1) * 64];
#pragma unroll
            for (int nt = 0; nt < 3; ++nt) {
                const u32x4 zf = *(const u32x4*)(smem + L_Z + zaddr(nt * 16 + c16, 4 * ks + g));
                acc[0][nt] = mfma16(w0, zf, acc[0][nt]);
                acc[1][nt] = mfma16(w1, zf, acc[1][nt]);
            }
        }
#pragma unroll
        for (int nt = 0; nt < 3; ++nt) {
            const int nl = nt * 16 + c16;
            if (nl < T) {
                u32x4 o;
                o[0] = pack_bf16(acc[0][nt][0], acc[0][nt][1]);
                o[1] = pack_bf16(acc[0][nt][2], acc[0][nt][3]);
                o[2] = pack_bf16(acc[1][nt][0], acc[1][nt][1]);
                o[3] = pack_bf16(acc[1][nt][2], acc[1][nt][3]);
                *(u32x4*)(out + (size_t)(n0 + nl) * DD + 32 * wv + 8 * g) = o;
            }
        }
    }
}

extern "C" void kernel_launch(void* const* d_in, const int* in_sizes, int n_in, void* d_out, int out_size,
                              void* d_ws, size_t ws_size, hipStream_t stream) {
    const unsigned short* x = (const unsigned short*)d_in[0];
    const int* src = (const int*)d_in[1];
    const int* dst = (const int*)d_in[2];
    const unsigned short* w = (const unsigned short*)d_in[3];
    const unsigned short* a_src = (const unsigned short*)d_in[4];
    const unsigned short* a_dst = (const unsigned short*)d_in[5];
    (void)hipFuncSetAttribute((const void*)gat_k2, hipFuncAttributeMaxDynamicSharedMemorySize, L_TOTAL);
    gat_k1<<<K1_GRID, 256, 0, stream>>>(x, src, dst, w, a_src, a_dst, (char*)d_ws);
    gat_k2<<<NT, 512, L_TOTAL, stream>>>(x, src, dst, (const char*)d_ws, (unsigned short*)d_out);
}
```

```cpp
#include <hip/hip_runtime.h>
#include <hip/hip_bf16.h>
#include <stdint.h>

typedef __attribute__((ext_vector_type(8))) __bf16 bf16x8;
typedef __attribute__((ext_vector_type(2))) __bf16 bf16x2;
typedef __attribute__((ext_vector_type(4))) short s16x4;
typedef __attribute__((ext_vector_type(4))) float f32x4;
typedef __attribute__((ext_vector_type(2))) float f32x2;
typedef __attribute__((ext_vector_type(4))) unsigned u32x4;
typedef __attribute__((ext_vector_type(2))) unsigned u32x2;
typedef __attribute__((ext_vector_type(2))) int i32x2;

constexpr int NN = 10000, NE = 160000, DD = 256, HH = 4;
constexpr int T = 40, NT = NN / T;
constexpr int NB = 64, CAP = 32, EPB = NE / NB;
constexpr int NG = T / 4;
constexpr int MAXQ = 56;
constexpr int SBT = 27;

constexpr size_t WS_CELLS = 0;
constexpr size_t WS_WT    = 2097152;
constexpr size_t WS_SSRC  = WS_WT + 524288;
constexpr size_t WS_SDST  = WS_SSRC + 655360;
constexpr size_t WS_CTAB  = WS_SDST + 655360;

__device__ __forceinline__ unsigned pack_bf16(float a, float b) {
    f32x2 v = {a, b};
    bf16x2 r = __builtin_convertvector(v, bf16x2);
    return __builtin_bit_cast(unsigned, r);
}
__device__ __forceinline__ float bf16_round(float a) {
    __bf16 r = (__bf16)a;
    return (float)r;
}
__device__ __forceinline__ f32x4 mfma16(u32x4 a, u32x4 b, f32x4 c) {
    return __builtin_amdgcn_mfma_f32_16x16x32_bf16(__builtin_bit_cast(bf16x8, a), __builtin_bit_cast(bf16x8, b), c, 0, 0, 0);
}
__device__ __forceinline__ f32x4 mfma16h(u32x2 a, u32x2 b, f32x4 c) {
    return __builtin_amdgcn_mfma_f32_16x16x16bf16_1k(__builtin_bit_cast(s16x4, a), __builtin_bit_cast(s16x4, b), c, 0, 0, 0);
}
__device__ __forceinline__ unsigned perm_lo(unsigned a, unsigned b) { return __builtin_amdgcn_perm(b, a, 0x05040100u); }
__device__ __forceinline__ unsigned perm_hi(unsigned a, unsigned b) { return __builtin_amdgcn_perm(b, a, 0x07060302u); }
__device__ __forceinline__ unsigned perm_sel(unsigned a, unsigned b, int hi) { return hi ? perm_hi(a, b) : perm_lo(a, b); }

template <int CTRL>
__device__ __forceinline__ float dpp_f(float v) {
    return __builtin_bit_cast(float, __builtin_amdgcn_update_dpp(0, __builtin_bit_cast(int, v), CTRL, 0xF, 0xF, true));
}
__device__ __forceinline__ float quad_transpose_sum(f32x4 v, int j) {
    const bool b0 = j & 1, b1 = j & 2;
    float t0 = b0 ? v[1] : v[0], s0 = b0 ? v[0] : v[1];
    float t1 = b0 ? v[3] : v[2], s1 = b0 ? v[2] : v[3];
    t0 += dpp_f<0xB1>(s0);
    t1 += dpp_f<0xB1>(s1);
    float keep = b1 ? t1 : t0, send = b1 ? t0 : t1;
    return keep + dpp_f<0x4E>(send);
}
__device__ __forceinline__ unsigned wave_incl_scan(unsigned v) {
    int x = (int)v;
    x += __builtin_amdgcn_update_dpp(0, x, 0x111, 0xF, 0xF, true);
    x += __builtin_amdgcn_update_dpp(0, x, 0x112, 0xF, 0xF, true);
    x += __builtin_amdgcn_update_dpp(0, x, 0x114, 0xF, 0xF, true);
    x += __builtin_amdgcn_update_dpp(0, x, 0x118, 0xF, 0xF, true);
    x += __builtin_amdgcn_update_dpp(0, x, 0x142, 0xA, 0xF, false);
    x += __builtin_amdgcn_update_dpp(0, x, 0x143, 0xC, 0xF, false);
    return (unsigned)x;
}

#define STAMP_DECL
#define STAMP_AT(idx)
#define STAMP_RT(idx)
#define STAMP_FLUSH(wsp, kid)

#define LDS_BARRIER() do { asm volatile("s_waitcnt lgkmcnt(0)" ::: "memory"); __builtin_amdgcn_s_barrier(); asm volatile("" ::: "memory"); } while (0)
__device__ __forceinline__ void dma16(const void* gsrc, void* lds_wave_base) {
    __builtin_amdgcn_global_load_lds((const unsigned*)gsrc, (__attribute__((address_space(3))) unsigned*)lds_wave_base, 16, 0, 0);
}

constexpr int K1L_W = 0;
constexpr int K1L_X = 65536;
constexpr int K1L_A = K1L_X + SBT * 2048;
constexpr int K1L_WA = K1L_A + 8192;
constexpr int K1L_TOTAL = K1L_WA + 1024;
constexpr int K1L_ST = 0;
constexpr int K1L_TC = NT * CAP * 4 + 256;

__global__ __launch_bounds__(512, 2) void gat_k1(const unsigned short* __restrict__ x, const int* __restrict__ src,
                                                 const int* __restrict__ dst, const unsigned short* __restrict__ w,
                                                 const unsigned short* __restrict__ a_src,
                                                 const unsigned short* __restrict__ a_dst, char* __restrict__ ws) {
    extern __shared__ __attribute__((aligned(16))) char smem[];
    const int tid = threadIdx.x, lane = tid & 63;
    const int wv = __builtin_amdgcn_readfirstlane(tid >> 6);
    const int b = blockIdx.x;
    STAMP_DECL STAMP_RT(14); STAMP_AT(0);
    if (b >= NB) {
        const int sb = b - NB;
        const int hp = sb / 96, q = (sb / 24) & 3, ib = sb % 24;
        const int n0 = ib * (SBT * 16);
        const int m = lane & 15, kc = lane >> 4;
        const int hl = wv >> 2, h = 2 * hp + hl;
        char* lw = smem + K1L_W + wv * 8192;
        char* la = smem + K1L_A + wv * 1024;
        dma16((lane < 32 ? a_src : a_dst) + h * DD + (lane & 31) * 8, la);
        {
            const unsigned short* wbase = w + ((size_t)h * DD + 64 * q + 16 * (wv & 3)) * DD;
#pragma unroll
            for (int i = 0; i < 8; ++i) {
                const int row = 2 * i + (lane >> 5);
                dma16(wbase + row * DD + (((lane & 31) ^ row) * 8), lw + i * 1024);
            }
        }
        __builtin_amdgcn_s_barrier();
#pragma unroll
        for (int r = 0; r < 4; ++r) {
            if (r < 3 || wv + 24 < SBT) {
#pragma unroll
                for (int i = 0; i < 2; ++i) {
                    const int nl = 8 * i + (lane >> 3);
                    int node = n0 + (wv + 8 * r) * 16 + nl;
                    node = node < NN ? node : NN - 1;
                    dma16(x + (size_t)node * DD + 64 * q + (((lane & 7) ^ ((nl >> 1) & 7)) * 8), smem + K1L_X + (wv + 8 * r) * 2048 + i * 1024);
                }
            }
        }
        if (wv + 24 < SBT) asm volatile("s_waitcnt vmcnt(8)" ::: "memory");
        else asm volatile("s_waitcnt vmcnt(6)" ::: "memory");
        __builtin_amdgcn_sched_barrier(0);
        STAMP_AT(1);
        {
            f32x4 acc0 = {0.f, 0.f, 0.f, 0.f}, acc1 = {0.f, 0.f, 0.f, 0.f};
#pragma unroll
            for (int ks = 0; ks < 8; ks += 2) {
                const u32x4 b0 = *(const u32x4*)(la + (m & 1) * 512 + (4 * ks + kc) * 16);
                const u32x4 b1 = *(const u32x4*)(la + (m & 1) * 512 + (4 * ks + 4 + kc) * 16);
                const u32x4 a0 = *(const u32x4*)(lw + m * 512 + (((4 * ks + kc) ^ m) * 16));
                const u32x4 a1 = *(const u32x4*)(lw + m * 512 + (((4 * ks + 4 + kc) ^ m) * 16));
                acc0 = mfma16(a0, b0, acc0);
                acc1 = mfma16(a1, b1, acc1);
            }
            acc0 += acc1;
            u32x2 hi, lo;
            hi[0] = pack_bf16(acc0[0], acc0[1]); hi[1] = pack_bf16(acc0[2], acc0[3]);
            lo[0] = pack_bf16(acc0[0] - __uint_as_float(hi[0] << 16), acc0[1] - __uint_as_float(hi[0] & 0xFFFF0000u));
            lo[1] = pack_bf16(acc0[2] - __uint_as_float(hi[1] << 16), acc0[3] - __uint_as_float(hi[1] & 0xFFFF0000u));
            if (m < 2) {
                char* p = smem + K1L_WA + (m * 2 + hl) * 128 + (16 * (wv & 3) + 4 * kc) * 2;
                *(u32x2*)p = hi;
                *(u32x2*)(p + 512) = lo;
            }
        }
        LDS_BARRIER();
        STAMP_AT(2);
        u32x4 ah[2][2];
#pragma unroll
        for (int k2 = 0; k2 < 2; ++k2)
#pragma unroll
            for (int sp = 0; sp < 2; ++sp)
                ah[k2][sp] = *(const u32x4*)(smem + K1L_WA + sp * 512 + (m & 3) * 128 + (32 * k2 + 8 * kc) * 2);
        asm volatile("s_waitcnt vmcnt(0)" ::: "memory");
        __builtin_amdgcn_sched_barrier(0);
        float* sout = (float*)(ws + (kc == 0 ? WS_SSRC : WS_SDST)) + q * 4 + 2 * hp;
        f32x4 acc[4];
#pragma unroll
        for (int r = 0; r < 4; ++r) {
            const int tt = (r < 3 || wv + 24 < SBT) ? wv + 8 * r : wv;
            const u32x4 x0 = *(const u32x4*)(smem + K1L_X + tt * 2048 + m * 128 + ((kc ^ ((m >> 1) & 7)) * 16));
            const u32x4 x1 = *(const u32x4*)(smem + K1L_X + tt * 2048 + m * 128 + (((4 + kc) ^ ((m >> 1) & 7)) * 16));
            acc[r] = f32x4{0.f, 0.f, 0.f, 0.f};
            acc[r] = mfma16(ah[0][1], x0, acc[r]);
            acc[r] = mfma16(ah[1][1], x1, acc[r]);
            acc[r] = mfma16(ah[0][0], x0, acc[r]);
            acc[r] = mfma16(ah[1][0], x1, acc[r]);
        }
#pragma unroll
        for (int r = 0; r < 4; ++r) {
            const int node = n0 + (wv + 8 * r) * 16 + m;
            if (kc < 2 && node < NN && wv + 8 * r < SBT) {
                const f32x2 o = kc == 0 ? f32x2{acc[r][0], acc[r][1]} : f32x2{acc[r][2], acc[r][3]};
                *(f32x2*)(sout + (size_t)node * 16) = o;
            }
        }
    } else {
        const int c = b;
        unsigned* stage = (unsigned*)(smem + K1L_ST);
        unsigned long long* tcnt = (unsigned long long*)(smem + K1L_TC);
        if (wv < 7) {
            const i32x2* d2 = (const i32x2*)(dst + c * EPB);
            const i32x2* s2 = (const i32x2*)(src + c * EPB);
            i32x2 dv[3], sv[3];
#pragma unroll
            for (int r = 0; r < 3; ++r) {
                const int i = tid + 448 * r;
                const int ii = i < EPB / 2 ? i : 0;
                dv[r] = d2[ii]; sv[r] = s2[ii];
            }
            __builtin_amdgcn_sched_barrier(0);
            if (tid < 256) tcnt[tid] = 0ull;
            LDS_BARRIER();
            __builtin_amdgcn_sched_barrier(0);
            STAMP_AT(1);
            unsigned long long old[6]; unsigned tt[6], dl[6];
#pragma unroll
            for (int k = 0; k < 6; ++k) {
                const unsigned d = (unsigned)dv[k >> 1][k & 1];
                tt[k] = d / (unsigned)T;
                dl[k] = d - tt[k] * T;
            }
#pragma unroll
            for (int k = 0; k < 4; ++k) old[k] = atomicAdd(&tcnt[tt[k]], (1ull << 50) | (1ull << (5 * (dl[k] >> 2))));
            old[4] = old[5] = ~0ull;
            if (tid + 896 < EPB / 2) {
#pragma unroll
                for (int k = 4; k < 6; ++k) old[k] = atomicAdd(&tcnt[tt[k]], (1ull << 50) | (1ull << (5 * (dl[k] >> 2))));
            }
            unsigned ent[6];
#pragma unroll
            for (int k = 0; k < 6; ++k) {
                const unsigned grp = dl[k] >> 2;
                const unsigned j = (grp * 11u) >> 5, sh = (grp - 3u * j) * 10u;
                ent[k] = (unsigned)sv[k >> 1][k & 1] | (dl[k] << 14) | (j << 25) | (sh << 27);
            }
            __builtin_amdgcn_sched_barrier(0);
#pragma unroll
            for (int k = 0; k < 6; ++k) {
                const unsigned pos = (unsigned)(old[k] >> 50);
                const unsigned rank = (unsigned)(old[k] >> (5 * (dl[k] >> 2))) & 31u;
                const unsigned idx = pos < (unsigned)CAP ? tt[k] * CAP + pos : (unsigned)(NT * CAP + lane);
                stage[idx] = ent[k] | (rank << 20);
            }
        } else {
            const int ks = c >> 1;
            const int kc = 2 * (c & 1) + (lane >> 5), fc = lane & 31;
            u32x4 r[8];
#pragma unroll
            for (int j = 0; j < 8; ++j) r[j] = *(const u32x4*)(w + (size_t)(32 * ks + 8 * kc + j) * DD + 8 * fc);
            __builtin_amdgcn_sched_barrier(0);
            __builtin_amdgcn_s_barrier();
            __builtin_amdgcn_sched_barrier(0);
            const int wq = fc >> 2, gp = fc & 3;
            char* wt = ws + WS_WT + (size_t)((wq * 32 + ks) * 2) * 1024 + (kc * 16 + 4 * gp) * 16;
#pragma unroll
            for (int i = 0; i < 8; ++i) {
                u32x4 o;
#pragma unroll
                for (int p = 0; p < 4; ++p) o[p] = perm_sel(r[2 * p][i >> 1], r[2 * p + 1][i >> 1], i & 1);
                *(u32x4*)(wt + (i >> 2) * 1024 + (i & 3) * 16) = o;
            }
        }
        LDS_BARRIER();
        STAMP_AT(2);
        u32x4* cells4 = (u32x4*)(ws + WS_CELLS);
        u32x4 v[4]; unsigned tc4[4];
#pragma unroll
        for (int i = 0; i < 4; ++i) {
            const int ii = tid + 512 * i < NT * CAP / 4 ? tid + 512 * i : 0;
            v[i] = ((u32x4*)stage)[ii];
            tc4[i] = ((const unsigned*)tcnt)[(ii >> 3) * 2 + 1] >> 18;
        }
        const unsigned long long cw = tcnt[tid & 255];
#pragma unroll
        for (int i = 0; i < 4; ++i) {
            const int ii = tid + 512 * i;
            if (ii < NT * CAP / 4 && (unsigned)((ii & 7) * 4) < tc4[i]) cells4[((ii >> 3) * NB + c) * 8 + (ii & 7)] = v[i];
        }
        if (tid < NT) {
            unsigned tc = (unsigned)(cw >> 50);
            const unsigned bad = (tc > 31u || (cw & 0x2108421084210ull) != 0ull) ? 1u : 0u;
            tc = tc > 31u ? 31u : tc;
            unsigned gc[NG];
#pragma unroll
            for (int k = 0; k < NG; ++k) gc[k] = (unsigned)(cw >> (5 * k)) & 15u;
            u32x4 o;
            o[0] = gc[0] | (gc[1] << 10) | (gc[2] << 20);
            o[1] = gc[3] | (gc[4] << 10) | (gc[5] << 20);
            o[2] = gc[6] | (gc[7] << 10) | (gc[8] << 20);
            o[3] = gc[9] | (tc << 10) | (bad << 24);
            ((u32x4*)(ws + WS_CTAB))[tid * NB + c] = o;
        }
    }
    STAMP_AT(3); STAMP_RT(15); STAMP_FLUSH(ws, 0);
}

constexpr int L_Z = 0;
constexpr int L_P = L_Z + T * 2048;
constexpr int L_EL = L_P + MAXQ * 512;
constexpr int L_SD = L_EL + (MAXQ + 8) * 64 + 256;
constexpr int L_TOTAL = L_SD + T * 16;
constexpr unsigned OOB = 0x80000000u;
#define WIN 28
#define WPRE 4

__device__ __forceinline__ int zaddr(int node, int ch) {
    return node * 2048 + 16 * (ch ^ ((node ^ (2 * (ch >> 5))) & 15));
}

__global__ __launch_bounds__(512, 2) void gat_k2(const unsigned short* __restrict__ x, const int* __restrict__ src,
                                                 const int* __restrict__ dst, const char* __restrict__ ws,
                                                 unsigned short* __restrict__ out) {
    extern __shared__ __attribute__((aligned(16))) char smem[];
    const int tid = threadIdx.x, lane = tid & 63;
    const int wv = __builtin_amdgcn_readfirstlane(tid >> 6);
    const int tile = blockIdx.x;
    const int n0 = tile * T;
    unsigned* elist = (unsigned*)(smem + L_EL);
    float* sd = (float*)(smem + L_SD);
    const float* ssrc = (const float*)(ws + WS_SSRC);
    const float* sdst = (const float*)(ws + WS_SDST);
    const int c16 = lane & 15, g = lane >> 4;

    STAMP_DECL STAMP_RT(14); STAMP_AT(0);
    const u32x4 ct = ((const u32x4*)(ws + WS_CTAB))[tile * NB + lane];
    const u32x4 ce = ((const u32x4*)(ws + WS_CELLS))[tile * (NB * CAP / 4) + tid];
    f32x4 sdv = {0.f, 0.f, 0.f, 0.f};
    if (wv < 3) sdv = *(const f32x4*)(sdst + (size_t)n0 * 16 + (tid < T * 4 ? tid : 0) * 4);
    __builtin_amdgcn_sched_barrier(0);
    {
        const u32x4 z4 = {0u, 0u, 0u, 0u};
#pragma unroll
        for (int i = 0; i < 4; ++i)
            if (tid + 512 * i < MAXQ * 32) ((u32x4*)(smem + L_P))[tid + 512 * i] = z4;
        const u32x4 o4 = {OOB, OOB, OOB, OOB};
        if (tid < (MAXQ + 8) * 4) ((u32x4*)elist)[tid] = o4;
    }
    LDS_BARRIER();
    const u32x4* wt = (const u32x4*)(ws + WS_WT) + (size_t)wv * 64 * 64 + lane;
    u32x4 wr[WIN];
#pragma unroll
    for (int i = 0; i < WPRE; ++i) wr[i] = wt[i * 64];
    __builtin_amdgcn_sched_barrier(0);
    u32x4 inc;
#pragma unroll
    for (int k = 0; k < 4; ++k) inc[k] = wave_incl_scan(ct[k]);
    unsigned tot[4];
#pragma unroll
    for (int k = 0; k < 4; ++k) tot[k] = (unsigned)__builtin_amdgcn_readlane((int)inc[k], 63);
    int q0[NG + 1];
    q0[0] = 0;
#pragma unroll
    for (int k = 0; k < NG; ++k) {
        const int nqk = (int)((((tot[k / 3] >> (10 * (k % 3))) & 1023u) + 15u) >> 4);
        q0[k + 1] = q0[k] + (nqk > 1 ? nqk : 1);
    }
    const bool slow = (tot[3] >> 24) != 0u || q0[NG] > MAXQ;
    STAMP_AT(1);
    {
        unsigned pre0, pre1, pre2, pre3, ccnt;
        {
            unsigned ex[4];
#pragma unroll
            for (int k = 0; k < 4; ++k) {
                unsigned base = 0;
#pragma unroll
                for (int f = 0; f < 3; ++f) if (3 * k + f < NG) base |= (unsigned)(q0[3 * k + f] * 16) << (10 * f);
                ex[k] = inc[k] - ct[k] + base;
            }
            ex[3] = (ex[3] & 1023u) | (ct[3] & (63u << 10));
            const int sl = (wv * 8 + (lane >> 3)) * 4;
            pre0 = (unsigned)__builtin_amdgcn_ds_bpermute(sl, (int)ex[0]);
            pre1 = (unsigned)__builtin_amdgcn_ds_bpermute(sl, (int)ex[1]);
            pre2 = (unsigned)__builtin_amdgcn_ds_bpermute(sl, (int)ex[2]);
            pre3 = (unsigned)__builtin_amdgcn_ds_bpermute(sl, (int)ex[3]);
            asm volatile("" : "+v"(pre0), "+v"(pre1), "+v"(pre2), "+v"(pre3));
            ccnt = slow ? 0u : (pre3 >> 10);
        }
#pragma unroll
        for (int k = 0; k < 4; ++k) {
            const unsigned e = ce[k];
            const bool j0 = (e >> 25) & 1u, j1 = (e >> 26) & 1u;
            const unsigned lo2 = j0 ? pre1 : pre0, hi2 = j0 ? pre3 : pre2;
            const unsigned dw = j1 ? hi2 : lo2;
            const unsigned slot = ((dw >> (e >> 27)) & 1023u) + ((e >> 20) & 31u);
            unsigned pos = (slot & ~15u) | ((slot & 3u) << 2) | ((slot >> 2) & 3u);
            pos = ((unsigned)((lane & 7) * 4 + k) < ccnt) ? pos : (unsigned)((MAXQ + 8) * 16 + lane);
            elist[pos] = ((e & 0x3FFFu) << 9) | ((e >> 14) & 63u);
        }
        const float sq = quad_transpose_sum(sdv, tid & 3);
        if (tid < T * 4) sd[tid] = sq;
    }
    LDS_BARRIER();
    STAMP_AT(2);

    if (!slow) {
#define ABL_X (NN * DD * 2)
#define ABL_S (NN * 64)
        const u32x4 xr = {(unsigned)(uintptr_t)x, (unsigned)((uintptr_t)x >> 32) & 0xFFFFu, (unsigned)(ABL_X), 0x00020000u};
        const u32x4 sr = {(unsigned)(uintptr_t)ssrc, (unsigned)((uintptr_t)ssrc >> 32) & 0xFFFFu, (unsigned)(ABL_S), 0x00020000u};
        const u32x4 ones = {0x3F803F80u, 0x3F803F80u, 0x3F803F80u, 0x3F803F80u};
        const u32x2 ones2 = {0x3F803F80u, 0x3F803F80u};
        const int j = tid & 3;
        constexpr int PR = MAXQ * 16 / 128;
        static_assert(PR == 7, "asm below assumes 7 rounds");
        unsigned fe[PR]; u32x4 sv[PR]; float sdl[PR];
#pragma unroll
        for (int r = 0; r < PR; ++r) fe[r] = elist[(tid >> 2) + 128 * r];
        {
            unsigned so[PR];
#pragma unroll
            for (int r = 0; r < PR; ++r) so[r] = ((fe[r] & ~511u) >> 3) | (unsigned)(j * 16);
            asm volatile("buffer_load_dwordx4 %0, %7, %14, 0 offen\n\tbuffer_load_dwordx4 %1, %8, %14, 0 offen\n\t"
                         "buffer_load_dwordx4 %2, %9, %14, 0 offen\n\tbuffer_load_dwordx4 %3, %10, %14, 0 offen\n\t"
                         "buffer_load_dwordx4 %4, %11, %14, 0 offen\n\tbuffer_load_dwordx4 %5, %12, %14, 0 offen\n\t"
                         "buffer_load_dwordx4 %6, %13, %14, 0 offen"
                         : "=&v"(sv[0]), "=&v"(sv[1]), "=&v"(sv[2]), "=&v"(sv[3]), "=&v"(sv[4]), "=&v"(sv[5]), "=&v"(sv[6])
                         : "v"(so[0]), "v"(so[1]), "v"(so[2]), "v"(so[3]), "v"(so[4]), "v"(so[5]), "v"(so[6]), "s"(sr));
        }
#pragma unroll
        for (int r = 0; r < PR; ++r) sdl[r] = sd[(fe[r] & 63u) * 4 + j];
        int iu = wv, iq, iqe;
        int sq[3], sn[3], su[3];
        u32x4 r[3][8];
#pragma unroll
        for (int a = 0; a < 3; ++a)
#pragma unroll
            for (int k = 0; k < 8; ++k) r[a][k] = u32x4{0u, 0u, 0u, 0u};
#define UNIT_RANGE() do { const int gg = iu >= NG ? iu - NG : iu; iq = 0; iqe = 0; \
        _Pragma("unroll") for (int k = 0; k < NG; ++k) if (gg == k) { iq = q0[k]; iqe = q0[k + 1]; } } while (0)
#define ISSUE(S) do { \
        const bool valid = iu < 2 * NG; \
        const int rem = iqe - iq; \
        sq[S] = iq; sn[S] = valid ? (rem < 2 ? rem : 2) : -1; \
        const bool last = rem <= 2; \
        su[S] = iu | (last ? 256 : 0); \
        const unsigned lo = c16 * 16 + (iu >= NG ? 256 : 0); \
        const u32x4 o0 = *(const u32x4*)(elist + iq * 16 + 4 * g), o1 = *(const u32x4*)(elist + iq * 16 + 16 + 4 * g); \
        if (sn[S] == 1) { _Pragma("unroll") for (int k = 4; k < 8; ++k) r[S][k] = u32x4{0u, 0u, 0u, 0u}; } \
        asm volatile("s_cmp_lt_i32 %17, 1\n\ts_cbranch_scc1 .Lskipq%=\n\t" \
                     "buffer_load_dwordx4 %0, %8, %16, 0 offen\n\tbuffer_load_dwordx4 %1, %9, %16, 0 offen\n\t" \
                     "buffer_load_dwordx4 %2, %10, %16, 0 offen\n\tbuffer_load_dwordx4 %3, %11, %16, 0 offen\n\t" \
                     "s_cmp_lt_i32 %17, 2\n\ts_cbranch_scc1 .Lskipq%=\n\t" \
                     "buffer_load_dwordx4 %4, %12, %16, 0 offen\n\tbuffer_load_dwordx4 %5, %13, %16, 0 offen\n\t" \
                     "buffer_load_dwordx4 %6, %14, %16, 0 offen\n\tbuffer_load_dwordx4 %7, %15, %16, 0 offen\n" \
                     ".Lskipq%=:" \
                     : "+v"(r[S][0]), "+v"(r[S][1]), "+v"(r[S][2]), "+v"(r[S][3]), "+v"(r[S][4]), "+v"(r[S][5]), "+v"(r[S][6]), "+v"(r[S][7]) \
                     : "v"((o0[0] & ~511u) | lo), "v"((o0[1] & ~511u) | lo), "v"((o0[2] & ~511u) | lo), "v"((o0[3] & ~511u) | lo), \
                       "v"((o1[0] & ~511u) | lo), "v"((o1[1] & ~511u) | lo), "v"((o1[2] & ~511u) | lo), "v"((o1[3] & ~511u) | lo), \
                       "s"(xr), "s"(sn[S]) : "scc"); \
        iq += 2; \
        if (valid && last) { iu += 8; UNIT_RANGE(); } } while (0)
#define WAITQ(y) asm volatile("s_cmp_lt_i32 %0, 4\n\ts_cbranch_scc1 .Lw3%=\n\ts_waitcnt vmcnt(16)\n\ts_branch .Lwe%=\n" \
                     ".Lw3%=:\n\ts_cmp_lt_i32 %0, 3\n\ts_cbranch_scc1 .Lw2%=\n\ts_waitcnt vmcnt(12)\n\ts_branch .Lwe%=\n" \
                     ".Lw2%=:\n\ts_cmp_lt_i32 %0, 2\n\ts_cbranch_scc1 .Lw1%=\n\ts_waitcnt vmcnt(8)\n\ts_branch .Lwe%=\n" \
                     ".Lw1%=:\n\ts_cmp_lt_i32 %0, 1\n\ts_cbranch_scc1 .Lw0%=\n\ts_waitcnt vmcnt(4)\n\ts_branch .Lwe%=\n" \
                     ".Lw0%=:\n\ts_waitcnt vmcnt(0)\n" \
                     ".Lwe%=:" :: "s"(y) : "scc", "memory")
#define POS(v) ((v) > 0 ? (v) : 0)
#define COMPUTE(S, Y1, Y2) do { \
        WAITQ(POS(sn[Y1]) + POS(sn[Y2])); \
        asm volatile("" : "+v"(r[S][0]), "+v"(r[S][1]), "+v"(r[S][2]), "+v"(r[S][3]), "+v"(r[S][4]), "+v"(r[S][5]), "+v"(r[S][6]), "+v"(r[S][7])); \
        const char* pb = smem + L_P + sq[S] * 512 + lane * 8; \
        { \
            const u32x2 p0 = *(const u32x2*)pb, p1 = *(const u32x2*)(pb + 512); \
            const bool two = sn[S] == 2; \
            const u32x4 pf = {p0[0], p0[1], two ? p1[0] : 0u, two ? p1[1] : 0u}; \
            accd = mfma16(ones, pf, accd); \
            _Pragma("unroll") for (int i = 0; i < 8; ++i) { \
                u32x4 f; \
                _Pragma("unroll") for (int p = 0; p < 4; ++p) f[p] = perm_sel(r[S][2 * p][i >> 1], r[S][2 * p + 1][i >> 1], i & 1); \
                acc[i] = mfma16(f, pf, acc[i]); \
            } \
        } \
        const int fu = su[S]; \
        ISSUE(S); \
        if (fu & 256) { \
            const int uu = fu & 255, hf = uu >= NG ? 1 : 0, gg = uu - hf * NG; \
            const float sc = 0.25f / (accd[0] + 1e-9f); \
            const int node = gg * 4 + (c16 >> 2), h = c16 & 3; \
            _Pragma("unroll") for (int reg = 0; reg < 4; ++reg) { \
                u32x4 o; \
                _Pragma("unroll") for (int p = 0; p < 4; ++p) o[p] = pack_bf16(acc[2 * p][reg] * sc, acc[2 * p + 1][reg] * sc); \
                *(u32x4*)(smem + L_Z + zaddr(node, h * 32 + hf * 16 + 4 * g + reg)) = o; \
            } \
            accd = f32x4{0.f, 0.f, 0.f, 0.f}; \
            _Pragma("unroll") for (int i = 0; i < 8; ++i) acc[i] = f32x4{0.f, 0.f, 0.f, 0.f}; \
        } } while (0)
        UNIT_RANGE();
        ISSUE(0); ISSUE(1);
        WAITQ(POS(sn[0]) + POS(sn[1]));
        asm volatile("" : "+v"(sv[0]), "+v"(sv[1]), "+v"(sv[2]), "+v"(sv[3]), "+v"(sv[4]), "+v"(sv[5]), "+v"(sv[6]));
#pragma unroll
        for (int q = 0; q < PR; ++q) {
            const float ss = quad_transpose_sum(__builtin_bit_cast(f32x4, sv[q]), j);
            float e = ss + sdl[q];
            e = e > 0.f ? e : 0.2f * e;
            float p = __expf(e);
            p = fe[q] != OOB ? p : 0.f;
            const __bf16 pb = (__bf16)p;
            const unsigned pos = (tid >> 2) + 128 * q;
            *(__bf16*)(smem + L_P + (pos >> 2) * 128 + ((fe[q] & 3u) * 4 + j) * 8 + (pos & 3) * 2) = pb;
        }
        ISSUE(2);
        STAMP_AT(3);
        LDS_BARRIER();
        STAMP_AT(4);
        f32x4 acc[8], accd = {0.f, 0.f, 0.f, 0.f};
#pragma unroll
        for (int i = 0; i < 8; ++i) acc[i] = f32x4{0.f, 0.f, 0.f, 0.f};
#pragma unroll 1
        while (true) {
            if (sn[0] < 0) break;
            COMPUTE(0, 1, 2);
            if (sn[1] < 0) break;
            COMPUTE(1, 2, 0);
            if (sn[2] < 0) break;
            COMPUTE(2, 0, 1);
        }
        asm volatile("s_waitcnt vmcnt(0)" ::: "memory");
    } else {
#pragma unroll 1
        for (int nl = wv; nl < T; nl += 8) {
            const int node = n0 + nl;
            float acc[4][4], den[4];
#pragma unroll
            for (int h = 0; h < 4; ++h) { den[h] = 0.f;
#pragma unroll
                for (int i = 0; i < 4; ++i) acc[h][i] = 0.f; }
#pragma unroll 1
            for (int e0 = 0; e0 < NE; e0 += 64) {
                const int e = e0 + lane;
                const int dv = dst[e];
                const int sv = src[e];
                unsigned long long mask = __ballot(dv == node);
                while (mask) {
                    const int bit = __builtin_ctzll(mask);
                    mask &= mask - 1;
                    const int s = __shfl(sv, bit, 64);
                    const f32x4 q0v = *(const f32x4*)(ssrc + (size_t)s * 16);
                    const f32x4 q1v = *(const f32x4*)(ssrc + (size_t)s * 16 + 4);
                    const f32x4 q2v = *(const f32x4*)(ssrc + (size_t)s * 16 + 8);
                    const f32x4 q3v = *(const f32x4*)(ssrc + (size_t)s * 16 + 12);
                    const uint2 xv = *(const uint2*)(x + (size_t)s * DD + lane * 4);
                    const float xf[4] = {__uint_as_float(xv.x << 16), __uint_as_float(xv.x & 0xFFFF0000u),
                                         __uint_as_float(xv.y << 16), __uint_as_float(xv.y & 0xFFFF0000u)};
#pragma unroll
                    for (int h = 0; h < 4; ++h) {
                        float ev = ((q0v[h] + q1v[h]) + (q2v[h] + q3v[h])) + sd[nl * 4 + h];
                        ev = ev > 0.f ? ev : 0.2f * ev;
                        const float p = bf16_round(__expf(ev));
                        den[h] += p;
#pragma unroll
                        for (int i = 0; i < 4; ++i) acc[h][i] += p * xf[i];
                    }
                }
            }
#pragma unroll
            for (int h = 0; h < 4; ++h) {
                const float sc = 0.25f / (den[h] + 1e-9f);
                uint2 o;
                o.x = pack_bf16(acc[h][0] * sc, acc[h][1] * sc);
                o.y = pack_bf16(acc[h][2] * sc, acc[h][3] * sc);
                *(uint2*)(smem + L_Z + zaddr(nl, h * 32 + (lane >> 1)) + (lane & 1) * 8) = o;
            }
        }
    }
    STAMP_AT(5);
#pragma unroll
    for (int i = WPRE; i < WIN; ++i) wr[i] = wt[i * 64];
    __builtin_amdgcn_sched_barrier(0);
    LDS_BARRIER();
    STAMP_AT(6);
    {
        f32x4 acc[2][3];
#pragma unroll
        for (int t = 0; t < 2; ++t)
#pragma unroll
            for (int nt = 0; nt < 3; ++nt) acc[t][nt] = f32x4{0.f, 0.f, 0.f, 0.f};
        u32x4 zf[2][3];
#pragma unroll
        for (int nt = 0; nt < 3; ++nt) zf[0][nt] = *(const u32x4*)(smem + L_Z + zaddr(nt * 16 + c16, g));
#pragma unroll
        for (int ks = 0; ks < 32; ++ks) {
            if (ks + 1 < 32) {
#pragma unroll
                for (int nt = 0; nt < 3; ++nt)
                    zf[(ks + 1) & 1][nt] = *(const u32x4*)(smem + L_Z + zaddr(nt * 16 + c16, 4 * (ks + 1) + g));
            }
            const u32x4 w0 = wr[(ks * 2) % WIN], w1 = wr[(ks * 2 + 1) % WIN];
#pragma unroll
            for (int nt = 0; nt < 3; ++nt) {
                acc[0][nt] = mfma16(w0, zf[ks & 1][nt], acc[0][nt]);
                acc[1][nt] = mfma16(w1, zf[ks & 1][nt], acc[1][nt]);
            }
            if (ks * 2 + WIN < 64) {
                wr[(ks * 2) % WIN] = wt[(ks * 2 + WIN) * 64];
                wr[(ks * 2 + 1) % WIN] = wt[(ks * 2 + 1 + WIN) * 64];
            }
            __builtin_amdgcn_sched_barrier(0);
        }
        STAMP_AT(7);
#pragma unroll
        for (int nt = 0; nt < 3; ++nt) {
            const int nl = nt * 16 + c16;
            if (nl < T) {
                u32x4 o;
                o[0] = pack_bf16(acc[0][nt][0], acc[0][nt][1]);
                o[1] = pack_bf16(acc[0][nt][2], acc[0][nt][3]);
                o[2] = pack_bf16(acc[1][nt][0], acc[1][nt][1]);
                o[3] = pack_bf16(acc[1][nt][2], acc[1][nt][3]);
                *(u32x4*)(out + (size_t)(n0 + nl) * DD + 32 * wv + 8 * g) = o;
            }
        }
    }
    STAMP_AT(8); STAMP_RT(15); STAMP_FLUSH(ws, 1);
}

extern "C" void kernel_launch(void* const* d_in, const int* in_sizes, int n_in, void* d_out, int out_size,
                              void* d_ws, size_t ws_size, hipStream_t stream) {
    const unsigned short* x = (const unsigned short*)d_in[0];
    const int* src = (const int*)d_in[1];
    const int* dst = (const int*)d_in[2];
    const unsigned short* w = (const unsigned short*)d_in[3];
    const unsigned short* a_src = (const unsigned short*)d_in[4];
    const unsigned short* a_dst = (const unsigned short*)d_in[5];
    (void)hipFuncSetAttribute((const void*)gat_k1, hipFuncAttributeMaxDynamicSharedMemorySize, K1L_TOTAL);
    (void)hipFuncSetAttribute((const void*)gat_k2, hipFuncAttributeMaxDynamicSharedMemorySize, L_TOTAL);
    gat_k1<<<256, 512, K1L_TOTAL, stream>>>(x, src, dst, w, a_src, a_dst, (char*)d_ws);
    gat_k2<<<NT, 512, L_TOTAL, stream>>>(x, src, dst, (const char*)d_ws, (unsigned short*)d_out);
}
```

```cpp
#include <hip/hip_runtime.h>
#include <hip/hip_bf16.h>
#include <stdint.h>

typedef __attribute__((ext_vector_type(8))) __bf16 bf16x8;
typedef __attribute__((ext_vector_type(2))) __bf16 bf16x2;
typedef __attribute__((ext_vector_type(4))) short s16x4;
typedef __attribute__((ext_vector_type(4))) float f32x4;
typedef __attribute__((ext_vector_type(2))) float f32x2;
typedef __attribute__((ext_vector_type(4))) unsigned u32x4;
typedef __attribute__((ext_vector_type(2))) unsigned u32x2;
typedef __attribute__((ext_vector_type(2))) int i32x2;
#define LAS __attribute__((address_space(3)))

constexpr int NN = 10000, NE = 160000, DD = 256, HH = 4;
constexpr int T = 40, NT = NN / T;
constexpr int NB = 64, CAP = 32, EPB = NE / NB;
constexpr int NG = T / 4;
constexpr int MAXQ = 58;

constexpr size_t WS_CELLS = 0;
constexpr size_t WS_WT    = 2097152;
constexpr size_t WS_WA    = WS_WT + 524288;
constexpr size_t WS_CTAB  = WS_WA + 8192;

__device__ __forceinline__ unsigned pack_bf16(float a, float b) {
    f32x2 v = {a, b};
    bf16x2 r = __builtin_convertvector(v, bf16x2);
    return __builtin_bit_cast(unsigned, r);
}
__device__ __forceinline__ float bf16_round(float a) {
    __bf16 r = (__bf16)a;
    return (float)r;
}
__device__ __forceinline__ f32x4 mfma16(u32x4 a, u32x4 b, f32x4 c) {
    return __builtin_amdgcn_mfma_f32_16x16x32_bf16(__builtin_bit_cast(bf16x8, a), __builtin_bit_cast(bf16x8, b), c, 0, 0, 0);
}
__device__ __forceinline__ f32x4 mfma16h(s16x4 a, u32x2 b, f32x4 c) {
    return __builtin_amdgcn_mfma_f32_16x16x16bf16_1k(a, __builtin_bit_cast(s16x4, b), c, 0, 0, 0);
}
__device__ __forceinline__ unsigned perm_lo(unsigned a, unsigned b) { return __builtin_amdgcn_perm(b, a, 0x05040100u); }
__device__ __forceinline__ unsigned perm_hi(unsigned a, unsigned b) { return __builtin_amdgcn_perm(b, a, 0x07060302u); }
__device__ __forceinline__ unsigned perm_sel(unsigned a, unsigned b, int hi) { return hi ? perm_hi(a, b) : perm_lo(a, b); }
__device__ __forceinline__ unsigned wave_incl_scan(unsigned v) {
    int x = (int)v;
    x += __builtin_amdgcn_update_dpp(0, x, 0x111, 0xF, 0xF, true);
    x += __builtin_amdgcn_update_dpp(0, x, 0x112, 0xF, 0xF, true);
    x += __builtin_amdgcn_update_dpp(0, x, 0x114, 0xF, 0xF, true);
    x += __builtin_amdgcn_update_dpp(0, x, 0x118, 0xF, 0xF, true);
    x += __builtin_amdgcn_update_dpp(0, x, 0x142, 0xA, 0xF, false);
    x += __builtin_amdgcn_update_dpp(0, x, 0x143, 0xC, 0xF, false);
    return (unsigned)x;
}

#define STAMP_DECL
#define STAMP_AT(idx)
#define STAMP_RT(idx)
#define STAMP_ACC(idx)
#define STAMP_FLUSH(wsp, kid)

#define LDS_BARRIER() do { asm volatile("s_waitcnt lgkmcnt(0)" ::: "memory"); __builtin_amdgcn_s_barrier(); asm volatile("" ::: "memory"); } while (0)
__device__ __forceinline__ void dma4(const void* gsrc, void* lds_wave_base) {
    __builtin_amdgcn_global_load_lds((const unsigned*)gsrc, (LAS unsigned*)lds_wave_base, 4, 0, 0);
}
__device__ __forceinline__ void dma16(const void* gsrc, void* lds_wave_base) {
    __builtin_amdgcn_global_load_lds((const unsigned*)gsrc, (LAS unsigned*)lds_wave_base, 16, 0, 0);
}
__device__ __forceinline__ unsigned lds_addr(const void* p) { return (unsigned)(uintptr_t)(LAS const char*)p; }

constexpr int K1L_ST = 0;
constexpr int K1L_TC = NT * CAP * 4 + 256;
constexpr int K1L_DUMP = K1L_TC + 2048;
constexpr int K1L_TOTAL = K1L_DUMP + 256;
#define K1COV 512

__global__ __launch_bounds__(512, 2) void gat_k1(const unsigned short* __restrict__ x, const int* __restrict__ src,
                                                 const int* __restrict__ dst, const unsigned short* __restrict__ w,
                                                 const unsigned short* __restrict__ a_src,
                                                 const unsigned short* __restrict__ a_dst, char* __restrict__ ws) {
    extern __shared__ __attribute__((aligned(16))) char smem[];
    const int tid = threadIdx.x, lane = tid & 63;
    const int wv = __builtin_amdgcn_readfirstlane(tid >> 6);
    const int b = blockIdx.x;
    STAMP_DECL STAMP_RT(14); STAMP_AT(0);
    if (b >= NB) {
        if (b < NB + 64 && wv == 0) {
            const int task = b - NB, h = task >> 4, rb = task & 15;
            const int m = lane & 15, kc = lane >> 4;
            char* lw = smem;
            char* la = smem + 8192;
            dma16((lane < 32 ? a_src : a_dst) + h * DD + (lane & 31) * 8, la);
            const unsigned short* wbase = w + ((size_t)h * DD + 16 * rb) * DD;
#pragma unroll
            for (int i = 0; i < 8; ++i) {
                const int row = 2 * i + (lane >> 5);
                dma16(wbase + row * DD + (((lane & 31) ^ row) * 8), lw + i * 1024);
            }
            asm volatile("s_waitcnt vmcnt(0)" ::: "memory");
            STAMP_AT(1);
            f32x4 acc0 = {0.f, 0.f, 0.f, 0.f}, acc1 = {0.f, 0.f, 0.f, 0.f};
#pragma unroll
            for (int ks = 0; ks < 8; ks += 2) {
                const u32x4 b0 = *(const u32x4*)(la + (m & 1) * 512 + (4 * ks + kc) * 16);
                const u32x4 b1 = *(const u32x4*)(la + (m & 1) * 512 + (4 * ks + 4 + kc) * 16);
                const u32x4 a0 = *(const u32x4*)(lw + m * 512 + (((4 * ks + kc) ^ m) * 16));
                const u32x4 a1 = *(const u32x4*)(lw + m * 512 + (((4 * ks + 4 + kc) ^ m) * 16));
                acc0 = mfma16(a0, b0, acc0);
                acc1 = mfma16(a1, b1, acc1);
            }
            acc0 += acc1;
            u32x2 hi, lo;
            hi[0] = pack_bf16(acc0[0], acc0[1]); hi[1] = pack_bf16(acc0[2], acc0[3]);
            lo[0] = pack_bf16(acc0[0] - __uint_as_float(hi[0] << 16), acc0[1] - __uint_as_float(hi[0] & 0xFFFF0000u));
            lo[1] = pack_bf16(acc0[2] - __uint_as_float(hi[1] << 16), acc0[3] - __uint_as_float(hi[1] & 0xFFFF0000u));
            if (m < 4) {
                const int d = 16 * rb + 4 * kc, cd = d >> 3;
                const int ks = cd & 7, kq = ((cd >> 4) & 1) | (((cd >> 3) & 1) << 1);
                const int mr = 4 * h + 2 * (m & 1) + (m >> 1);
                *(u32x2*)(ws + WS_WA + ks * 1024 + (kq * 16 + mr) * 16 + (d & 7) * 2) = (m >> 1) ? lo : hi;
            }
            STAMP_AT(2);
        } else if (b >= NB + 64 && b < NB + 80 && wv < 3) {
            unsigned long long pc; int off, len;
            asm volatile("s_getpc_b64 %0\n.Lpc%=:\n\ts_mov_b32 %1, gat_k2_head_label-.Lpc%=\n\ts_mov_b32 %2, gat_k2_tail_label-gat_k2_head_label"
                         : "=s"(pc), "=s"(off), "=s"(len));
            if (tid * 128 < len && len < 65536) dma4((const char*)(((pc + off) & ~127ull) + tid * 128), smem + K1L_DUMP);
        }
        if (tid < K1COV) {
            unsigned line = (unsigned)(b & 7) * 5000u + ((unsigned)(b - NB) >> 3) * K1COV + (unsigned)tid;
            line = line >= 40000u ? line - 40000u : line;
            dma4((const char*)x + (size_t)line * 128, smem + K1L_DUMP);
        }
    } else {
        const int c = b;
        unsigned* stage = (unsigned*)(smem + K1L_ST);
        unsigned long long* tcnt = (unsigned long long*)(smem + K1L_TC);
        if (wv < 7) {
            const i32x2* d2 = (const i32x2*)(dst + c * EPB);
            const i32x2* s2 = (const i32x2*)(src + c * EPB);
            i32x2 dv[3], sv[3];
#pragma unroll
            for (int r = 0; r < 3; ++r) {
                const int i = tid + 448 * r;
                const int ii = i < EPB / 2 ? i : 0;
                dv[r] = d2[ii]; sv[r] = s2[ii];
            }
            __builtin_amdgcn_sched_barrier(0);
            if (tid < 256) tcnt[tid] = 0ull;
            LDS_BARRIER();
            __builtin_amdgcn_sched_barrier(0);
            STAMP_AT(1);
            unsigned long long old[6]; unsigned tt[6], dl[6];
#pragma unroll
            for (int k = 0; k < 6; ++k) {
                const unsigned d = (unsigned)dv[k >> 1][k & 1];
                tt[k] = d / (unsigned)T;
                dl[k] = d - tt[k] * T;
            }
#pragma unroll
            for (int k = 0; k < 4; ++k) old[k] = atomicAdd(&tcnt[tt[k]], (1ull << 50) | (1ull << (5 * (dl[k] >> 2))));
            old[4] = old[5] = ~0ull;
            if (tid + 896 < EPB / 2) {
#pragma unroll
                for (int k = 4; k < 6; ++k) old[k] = atomicAdd(&tcnt[tt[k]], (1ull << 50) | (1ull << (5 * (dl[k] >> 2))));
            }
            unsigned ent[6];
#pragma unroll
            for (int k = 0; k < 6; ++k) {
                const unsigned grp = dl[k] >> 2;
                const unsigned j = (grp * 11u) >> 5, sh = (grp - 3u * j) * 10u;
                ent[k] = (unsigned)sv[k >> 1][k & 1] | (dl[k] << 14) | (j << 25) | (sh << 27);
            }
            __builtin_amdgcn_sched_barrier(0);
#pragma unroll
            for (int k = 0; k < 6; ++k) {
                const unsigned pos = (unsigned)(old[k] >> 50);
                const unsigned rank = (unsigned)(old[k] >> (5 * (dl[k] >> 2))) & 31u;
                const unsigned idx = pos < (unsigned)CAP ? tt[k] * CAP + pos : (unsigned)(NT * CAP + lane);
                stage[idx] = ent[k] | (rank << 20);
            }
        } else {
            const int ks = c >> 1;
            const int kc = 2 * (c & 1) + (lane >> 5), fc = lane & 31;
            u32x4 r[8];
#pragma unroll
            for (int j = 0; j < 8; ++j) r[j] = *(const u32x4*)(w + (size_t)(32 * ks + 16 * (j >> 2) + 4 * kc + (j & 3)) * DD + 8 * fc);
            __builtin_amdgcn_sched_barrier(0);
            __builtin_amdgcn_s_barrier();
            __builtin_amdgcn_sched_barrier(0);
            const int wq = fc >> 2, gp = fc & 3;
            char* wt = ws + WS_WT + (size_t)((wq * 32 + ks) * 2) * 1024 + (kc * 16 + 4 * gp) * 16;
#pragma unroll
            for (int i = 0; i < 8; ++i) {
                u32x4 o;
#pragma unroll
                for (int p = 0; p < 4; ++p) o[p] = perm_sel(r[2 * p][i >> 1], r[2 * p + 1][i >> 1], i & 1);
                *(u32x4*)(wt + (i >> 2) * 1024 + (i & 3) * 16) = o;
            }
        }
        LDS_BARRIER();
        STAMP_AT(2);
        u32x4* cells4 = (u32x4*)(ws + WS_CELLS);
        u32x4 v[4]; unsigned tc4[4];
#pragma unroll
        for (int i = 0; i < 4; ++i) {
            const int ii = tid + 512 * i < NT * CAP / 4 ? tid + 512 * i : 0;
            v[i] = ((u32x4*)stage)[ii];
            tc4[i] = ((const unsigned*)tcnt)[(ii >> 3) * 2 + 1] >> 18;
        }
        const unsigned long long cw = tcnt[tid & 255];
#pragma unroll
        for (int i = 0; i < 4; ++i) {
            const int ii = tid + 512 * i;
            if (ii < NT * CAP / 4 && (unsigned)((ii & 7) * 4) < tc4[i]) cells4[((ii >> 3) * NB + c) * 8 + (ii & 7)] = v[i];
        }
        if (tid < NT) {
            unsigned tc = (unsigned)(cw >> 50);
            const unsigned bad = (tc > 31u || (cw & 0x2108421084210ull) != 0ull) ? 1u : 0u;
            tc = tc > 31u ? 31u : tc;
            unsigned gc[NG];
#pragma unroll
            for (int k = 0; k < NG; ++k) gc[k] = (unsigned)(cw >> (5 * k)) & 15u;
            u32x4 o;
            o[0] = gc[0] | (gc[1] << 10) | (gc[2] << 20);
            o[1] = gc[3] | (gc[4] << 10) | (gc[5] << 20);
            o[2] = gc[6] | (gc[7] << 10) | (gc[8] << 20);
            o[3] = gc[9] | (tc << 10) | (bad << 24);
            ((u32x4*)(ws + WS_CTAB))[tid * NB + c] = o;
        }
    }
    asm volatile("s_waitcnt vmcnt(0)" ::: "memory");
    STAMP_AT(3); STAMP_RT(15); STAMP_FLUSH(ws, 0);
}

constexpr int NLF = 6;
constexpr int L_Z = 0;
constexpr int L_W = L_Z + T * 2048;
constexpr int L_WA = L_W + 8 * NLF * 1024;
constexpr int L_P = L_WA + 8192;
constexpr int L_EL = L_P + NG * 2048;
constexpr int L_TOTAL = L_EL + (MAXQ + 2) * 64 + 256;
static_assert(L_TOTAL <= 163840, "LDS budget");
constexpr unsigned OOB = 0x80000000u;
#define WIN 24

__device__ __forceinline__ int zaddr(int node, int ch) {
    return node * 2048 + 16 * (ch ^ ((node ^ (2 * (ch >> 5))) & 15));
}
__device__ __forceinline__ unsigned sw2(unsigned r) { return ((r & 7u) << 1) | (r >> 3); }

__global__ __launch_bounds__(640, 3) void gat_k2(const unsigned short* __restrict__ x, const int* __restrict__ src,
                                                 const int* __restrict__ dst, const char* __restrict__ ws,
                                                 unsigned short* __restrict__ out) {
    extern __shared__ __attribute__((aligned(16))) char smem[];
    asm volatile("gat_k2_head_label:");
    const int tid = threadIdx.x, lane = tid & 63;
    const int wv = __builtin_amdgcn_readfirstlane(tid >> 6);
    const int tile = blockIdx.x;
    const int n0 = tile * T;
    unsigned* elist = (unsigned*)(smem + L_EL);
    const int c16 = lane & 15, g = lane >> 4;
    char* gbuf = smem + L_Z + wv * 8192;
    const unsigned gbuf_a = lds_addr(smem) + L_Z + wv * 8192;
#define ABL_X (NN * DD * 2 + 4096)
    const uintptr_t xbase = (uintptr_t)x - 4096;
    const u32x4 xr = {(unsigned)xbase, (unsigned)(xbase >> 32) & 0xFFFFu, (unsigned)(ABL_X), 0x00020000u};
    const unsigned lch = (unsigned)(lane & 31) * 16u;

    STAMP_DECL STAMP_RT(14); STAMP_AT(0);
#define DMA2(gp, la) do { unsigned keep_; \
        asm volatile("s_mov_b32 %0, m0\n\ts_mov_b32 m0, %2\n\ts_nop 0\n\t" \
                     "global_load_lds_dwordx4 %1, off\n\tglobal_load_lds_dwordx4 %1, off offset:1024\n\t" \
                     "s_mov_b32 m0, %0" : "=&s"(keep_) : "v"(gp), "s"(la) : "memory"); } while (0)
    u32x4 ct, ce;
    const unsigned own_a = lds_addr(smem) + L_P + wv * 2048;
    {
        const u32x4* pct = (const u32x4*)(ws + WS_CTAB) + tile * NB + lane;
        const u32x4* pce = (const u32x4*)(ws + WS_CELLS) + tile * (NB * CAP / 4) + (tid & 511);
        asm volatile("global_load_dwordx4 %0, %2, off\n\tglobal_load_dwordx4 %1, %3, off"
                     : "=&v"(ct), "=&v"(ce) : "v"(pct), "v"(pce) : "memory");
    }
    if (wv >= 8) {
        const char* wap = ws + WS_WA + (wv - 8) * 4096 + lane * 16;
        const unsigned wal = lds_addr(smem) + L_WA + (wv - 8) * 4096;
        DMA2(wap, wal); DMA2(wap + 2048, wal + 2048);
    }
    {
        const u32x4 o4 = {OOB, OOB, OOB, OOB}, z4 = {0u, 0u, 0u, 0u};
        if (tid < (MAXQ + 2) * 4) ((u32x4*)elist)[tid] = o4;
#pragma unroll
        for (int i = 0; i < 8; ++i) *(u32x4*)(gbuf + i * 1024 + lane * 16) = z4;
    }
    LDS_BARRIER();
    {
        const unsigned r0 = (unsigned)(lane >> 5), r1 = r0 + 2u;
        const unsigned o0 = ((unsigned)(n0 + 4 * wv) + r0) * 512u + 4096u + (lch ^ (sw2(r0) << 4));
        const unsigned o1 = ((unsigned)(n0 + 4 * wv) + r1) * 512u + 3072u + (lch ^ (sw2(r1) << 4));
        unsigned keep;
        asm volatile("s_mov_b32 %0, m0\n\ts_mov_b32 m0, %4\n\ts_nop 0\n\t"
                     "buffer_load_dwordx4 %1, %3, 0 offen lds\n\tbuffer_load_dwordx4 %2, %3, 0 offen offset:1024 lds\n\t"
                     "s_mov_b32 m0, %0"
                     : "=&s"(keep) : "v"(o0), "v"(o1), "s"(xr), "s"(own_a) : "memory");
    }
    if (wv >= 8) {
        const char* wsrc = ws + WS_WT + (size_t)(wv - 8) * 4 * 65536 + lane * 16;
        const unsigned wl = lds_addr(smem) + L_W + (wv - 8) * 4 * NLF * 1024;
#pragma unroll
        for (int i = 0; i < 4 * NLF; i += 2) DMA2(wsrc + (i / NLF) * 65536 + (i % NLF) * 1024, wl + i * 1024);
    }
    asm volatile("s_cmp_lt_i32 %2, 8\n\ts_cbranch_scc1 .Lwa%=\n\ts_waitcnt vmcnt(%3)\n\ts_branch .Lwb%=\n"
                 ".Lwa%=:\n\ts_waitcnt vmcnt(2)\n.Lwb%=:"
                 : "+v"(ct), "+v"(ce) : "s"(wv), "i"(4 * NLF + 2) : "scc", "memory");
    u32x4 inc;
#pragma unroll
    for (int k = 0; k < 4; ++k) inc[k] = wave_incl_scan(ct[k]);
    unsigned tot[4];
#pragma unroll
    for (int k = 0; k < 4; ++k) tot[k] = (unsigned)__builtin_amdgcn_readlane((int)inc[k], 63);
    const bool slow = (tot[3] >> 24) != 0u || ((tot[3] >> 10) & 0x3FFFu) > (unsigned)(MAXQ * 16 - 15 * NG);
    unsigned base[4];
    {
        unsigned run = 0;
#pragma unroll
        for (int k = 0; k < 4; ++k) {
            const unsigned pad = ((tot[k] & (k < 3 ? 0x3FFFFFFFu : 0x3FFu)) + 0x00F03C0Fu) & 0x3F0FC3F0u;
            const unsigned incl = pad * 0x00100401u;
            base[k] = incl - pad + run * 0x00100401u;
            run += (incl >> 20) & 1023u;
        }
    }
    int qi, left;
    {
        const int jw = (wv * 11) >> 5, sh = (wv - 3 * jw) * 10;
        const unsigned bw = jw == 0 ? base[0] : (jw == 1 ? base[1] : (jw == 2 ? base[2] : base[3]));
        const unsigned tw = jw == 0 ? tot[0] : (jw == 1 ? tot[1] : (jw == 2 ? tot[2] : tot[3]));
        qi = (int)(((bw >> sh) & 1023u) >> 4);
        left = (int)((tw >> sh) & 1023u);
    }
    STAMP_AT(1);
    if (wv < 8) {
        unsigned pre0, pre1, pre2, pre3, ccnt;
        {
            unsigned ex[4];
#pragma unroll
            for (int k = 0; k < 4; ++k) ex[k] = inc[k] - ct[k] + base[k];
            ex[3] = (ex[3] & 1023u) | (ct[3] & (63u << 10));
            const int sl = (wv * 8 + (lane >> 3)) * 4;
            pre0 = (unsigned)__builtin_amdgcn_ds_bpermute(sl, (int)ex[0]);
            pre1 = (unsigned)__builtin_amdgcn_ds_bpermute(sl, (int)ex[1]);
            pre2 = (unsigned)__builtin_amdgcn_ds_bpermute(sl, (int)ex[2]);
            pre3 = (unsigned)__builtin_amdgcn_ds_bpermute(sl, (int)ex[3]);
            asm volatile("" : "+v"(pre0), "+v"(pre1), "+v"(pre2), "+v"(pre3));
            ccnt = slow ? 0u : (pre3 >> 10);
        }
#pragma unroll
        for (int k = 0; k < 4; ++k) {
            const unsigned e = ce[k];
            const bool j0 = (e >> 25) & 1u, j1 = (e >> 26) & 1u;
            const unsigned lo2 = j0 ? pre1 : pre0, hi2 = j0 ? pre3 : pre2;
            const unsigned dw = j1 ? hi2 : lo2;
            const unsigned slot = ((dw >> (e >> 27)) & 1023u) + ((e >> 20) & 31u);
            const unsigned r = slot & 15u;
            unsigned pos = (slot & ~15u) | ((r & 1u) << 3) | (r >> 1);
            pos = ((unsigned)((lane & 7) * 4 + k) < ccnt) ? pos : (unsigned)((MAXQ + 2) * 16 + lane);
            elist[pos] = (((e & 0x3FFFu) << 9) + 4096u - ((r & 6u) << 9)) | (sw2(r) << 4) | ((e >> 14) & 3u);
        }
    }
    unsigned rowa = gbuf_a + c16 * 512 + (g & 1) * 256 + ((sw2((unsigned)c16) ^ ((unsigned)(g >> 1) << 3)) << 4);
    const char* wap = smem + L_WA + lane * 16;
#define READ_ROWS() do { asm volatile("" : "+v"(rowa)); \
        _Pragma("unroll") for (int ks = 0; ks < 8; ++ks) xb[ks] = *(const LAS u32x4*)(uintptr_t)(rowa ^ ((unsigned)ks << 4)); } while (0)
#define WAF(k) (*(const u32x4*)(wap + (k) * 1024))
#define LOGITS(LG) do { f32x4 l0 = {0.f, 0.f, 0.f, 0.f}, l1 = {0.f, 0.f, 0.f, 0.f}; \
        u32x4 w0 = WAF(0), w1 = WAF(1); \
        __builtin_amdgcn_sched_barrier(0); \
        l0 = mfma16(w0, xb[0], l0); l1 = mfma16(w1, xb[1], l1); \
        u32x4 w2 = WAF(2), w3 = WAF(3); \
        __builtin_amdgcn_sched_barrier(0); \
        l0 = mfma16(w2, xb[2], l0); \
        u32x4 w4 = WAF(4), w5 = WAF(5); \
        __builtin_amdgcn_sched_barrier(0); \
        l1 = mfma16(w3, xb[3], l1); \
        u32x4 w6 = WAF(6), w7 = WAF(7); \
        __builtin_amdgcn_sched_barrier(0); \
        l0 = mfma16(w4, xb[4], l0); l1 = mfma16(w5, xb[5], l1); \
        l0 = mfma16(w6, xb[6], l0); l1 = mfma16(w7, xb[7], l1); \
        LG = l0 + l1; } while (0)
    u32x4 xb[8];
    LDS_BARRIER();
    STAMP_AT(2);
    if (slow) left = 0;
    const int epos = ((c16 & 1) << 3) | (c16 >> 1);
    unsigned fe, ao[8];
#define PREP() do { \
        const u32x4 e0 = *(const u32x4*)(elist + qi * 16 + (lane >> 5) * 8), e1 = *(const u32x4*)(elist + qi * 16 + (lane >> 5) * 8 + 4); \
        fe = elist[qi * 16 + epos]; \
        _Pragma("unroll") for (int k = 0; k < 4; ++k) { ao[k] = (e0[k] & ~15u) ^ lch; ao[4 + k] = (e1[k] & ~15u) ^ lch; } } while (0)
#define ISSUE() do { \
        unsigned keep; \
        asm volatile("s_waitcnt lgkmcnt(0)\n\ts_mov_b32 %0, m0\n\ts_mov_b32 m0, %10\n\t" \
                     "s_cmp_lt_i32 %11, 15\n\ts_cbranch_scc1 .Lp%=\n\t" \
                     "buffer_load_dwordx4 %1, %9, 0 offen lds\n\tbuffer_load_dwordx4 %2, %9, 0 offen offset:1024 lds\n\t" \
                     "buffer_load_dwordx4 %3, %9, 0 offen offset:2048 lds\n\tbuffer_load_dwordx4 %4, %9, 0 offen offset:3072 lds\n\t" \
                     "s_add_u32 m0, m0, 0x1000\n\ts_nop 0\n\t" \
                     "buffer_load_dwordx4 %5, %9, 0 offen lds\n\tbuffer_load_dwordx4 %6, %9, 0 offen offset:1024 lds\n\t" \
                     "buffer_load_dwordx4 %7, %9, 0 offen offset:2048 lds\n\tbuffer_load_dwordx4 %8, %9, 0 offen offset:3072 lds\n\t" \
                     "s_branch .Lq%=\n.Lp%=:\n\t" \
                     "s_cmp_lt_i32 %11, 1\n\ts_cbranch_scc1 .Lq%=\n\tbuffer_load_dwordx4 %1, %9, 0 offen lds\n\t" \
                     "s_cmp_lt_i32 %11, 3\n\ts_cbranch_scc1 .Lq%=\n\tbuffer_load_dwordx4 %2, %9, 0 offen offset:1024 lds\n\t" \
                     "s_cmp_lt_i32 %11, 5\n\ts_cbranch_scc1 .Lq%=\n\tbuffer_load_dwordx4 %3, %9, 0 offen offset:2048 lds\n\t" \
                     "s_cmp_lt_i32 %11, 7\n\ts_cbranch_scc1 .Lq%=\n\tbuffer_load_dwordx4 %4, %9, 0 offen offset:3072 lds\n\t" \
                     "s_add_u32 m0, m0, 0x1000\n\t" \
                     "s_cmp_lt_i32 %11, 9\n\ts_cbranch_scc1 .Lq%=\n\tbuffer_load_dwordx4 %5, %9, 0 offen lds\n\t" \
                     "s_cmp_lt_i32 %11, 11\n\ts_cbranch_scc1 .Lq%=\n\tbuffer_load_dwordx4 %6, %9, 0 offen offset:1024 lds\n\t" \
                     "s_cmp_lt_i32 %11, 13\n\ts_cbranch_scc1 .Lq%=\n\tbuffer_load_dwordx4 %7, %9, 0 offen offset:2048 lds\n" \
                     ".Lq%=:\n\ts_mov_b32 m0, %0" \
                     : "=&s"(keep) \
                     : "v"(ao[0]), "v"(ao[1]), "v"(ao[2]), "v"(ao[3]), "v"(ao[4]), "v"(ao[5]), "v"(ao[6]), "v"(ao[7]), \
                       "s"(xr), "s"(gbuf_a), "s"(left) : "scc", "memory"); } while (0)
    PREP();
    asm volatile("s_cmp_lt_i32 %0, 8\n\ts_cbranch_scc1 .Lwa%=\n\ts_waitcnt vmcnt(%1)\n\ts_branch .Lwb%=\n"
                 ".Lwa%=:\n\ts_waitcnt vmcnt(0)\n.Lwb%=:" :: "s"(wv), "i"(4 * NLF) : "scc", "memory");
    ISSUE();
    float sdq;
    {
        const unsigned oa = own_a + (c16 & 3) * 512 + (g & 1) * 256 + ((sw2((unsigned)(c16 & 3)) ^ ((unsigned)(g >> 1) << 3)) << 4);
#pragma unroll
        for (int ks = 0; ks < 8; ++ks) xb[ks] = *(const LAS u32x4*)(uintptr_t)(oa ^ ((unsigned)ks << 4));
        f32x4 lg; LOGITS(lg); sdq = lg[2] + lg[3];
    }
    STAMP_AT(3); STAMP_AT(4);

    if (!slow) {
        const s16x4 ones2 = {0x3F80, 0x3F80, 0x3F80, 0x3F80};
        char* pscr = smem + L_P + wv * 2048;
        f32x4 acc[16], accd = {0.f, 0.f, 0.f, 0.f};
#pragma unroll
        for (int i = 0; i < 16; ++i) acc[i] = f32x4{0.f, 0.f, 0.f, 0.f};
        const unsigned trow = 4u * g + ((unsigned)c16 >> 2), tp = (unsigned)c16 & 3u;
        unsigned tra = gbuf_a + trow * 512 + (tp & 1u) * 8 + ((sw2(trow) ^ (tp >> 1)) << 4);
#pragma unroll 1
        while (left > 0) {
            const unsigned fc = fe;
            const float sdl = __builtin_bit_cast(float, __builtin_amdgcn_ds_bpermute((int)((g * 16 + (fc & 3u)) * 4), __builtin_bit_cast(int, sdq)));
            *(u32x2*)(pscr + lane * 8) = u32x2{0u, 0u};
            qi += 1; left -= 16;
            PREP();
            asm volatile("s_waitcnt vmcnt(0)" ::: "memory");
            READ_ROWS();
            s16x4 tf[16];
            asm volatile("" : "+v"(tra));
#pragma unroll
            for (int t = 0; t < 8; ++t) {
                const unsigned ta = tra ^ ((unsigned)t << 5);
                tf[t] = __builtin_amdgcn_ds_read_tr16_b64_v4i16((LAS s16x4*)(uintptr_t)ta);
                tf[t + 8] = __builtin_amdgcn_ds_read_tr16_b64_v4i16((LAS s16x4*)(uintptr_t)(ta + 256));
            }
            ISSUE();
            f32x4 lg; LOGITS(lg);
            float e = (lg[0] + lg[1]) + sdl;
            e = e > 0.f ? e : 0.2f * e;
            float p = __expf(e);
            p = fc != OOB ? p : 0.f;
            *(__bf16*)(pscr + ((c16 >> 2) * 16 + (fc & 3u) * 4 + g) * 8 + (c16 & 3) * 2) = (__bf16)p;
            const u32x2 pf = *(const u32x2*)(pscr + lane * 8);
#pragma unroll
            for (int t = 0; t < 16; ++t) acc[t] = mfma16h(tf[t], pf, acc[t]);
            accd = mfma16h(ones2, pf, accd);
        }
        {
            const float sc = 0.25f * __builtin_amdgcn_rcpf(accd[0] + 1e-9f);
            const int node = wv * 4 + (c16 >> 2), h = c16 & 3;
#pragma unroll
            for (int u = 0; u < 8; ++u) {
                const f32x4 a = acc[2 * u] * sc, b2 = acc[2 * u + 1] * sc;
                u32x4 o;
                o[0] = pack_bf16(a[0], a[1]); o[1] = pack_bf16(a[2], a[3]);
                o[2] = pack_bf16(b2[0], b2[1]); o[3] = pack_bf16(b2[2], b2[3]);
                *(u32x4*)(smem + L_Z + zaddr(node, h * 32 + 4 * u + g)) = o;
            }
        }
    } else {
        float wsv[4][4];
        {
            const int d = 4 * lane, cd = d >> 3;
            const int ks = cd & 7, kq = ((cd >> 4) & 1) | (((cd >> 3) & 1) << 1);
#pragma unroll
            for (int h = 0; h < 4; ++h) {
                const u32x2 hi = *(const u32x2*)(ws + WS_WA + ks * 1024 + (kq * 16 + 4 * h) * 16 + (d & 7) * 2);
                const u32x2 lo = *(const u32x2*)(ws + WS_WA + ks * 1024 + (kq * 16 + 4 * h + 1) * 16 + (d & 7) * 2);
                wsv[h][0] = __uint_as_float(hi[0] << 16) + __uint_as_float(lo[0] << 16);
                wsv[h][1] = __uint_as_float(hi[0] & 0xFFFF0000u) + __uint_as_float(lo[0] & 0xFFFF0000u);
                wsv[h][2] = __uint_as_float(hi[1] << 16) + __uint_as_float(lo[1] << 16);
                wsv[h][3] = __uint_as_float(hi[1] & 0xFFFF0000u) + __uint_as_float(lo[1] & 0xFFFF0000u);
            }
        }
#pragma unroll 1
        for (int nn = 0; nn < 4; ++nn) {
            const int nl = wv * 4 + nn;
            const int node = n0 + nl;
            float acc[4][4], den[4], sdn[4];
#pragma unroll
            for (int h = 0; h < 4; ++h) { den[h] = 0.f; sdn[h] = __shfl(sdq, h * 16 + nn, 64);
#pragma unroll
                for (int i = 0; i < 4; ++i) acc[h][i] = 0.f; }
#pragma unroll 1
            for (int e0 = 0; e0 < NE; e0 += 64) {
                const int e = e0 + lane;
                const int dv = dst[e];
                const int sv = src[e];
                unsigned long long mask = __ballot(dv == node);
                while (mask) {
                    const int bit = __builtin_ctzll(mask);
                    mask &= mask - 1;
                    const int s = __shfl(sv, bit, 64);
                    const uint2 xv = *(const uint2*)(x + (size_t)s * DD + lane * 4);
                    const float xf[4] = {__uint_as_float(xv.x << 16), __uint_as_float(xv.x & 0xFFFF0000u),
                                         __uint_as_float(xv.y << 16), __uint_as_float(xv.y & 0xFFFF0000u)};
#pragma unroll
                    for (int h = 0; h < 4; ++h) {
                        float dsum = (xf[0] * wsv[h][0] + xf[1] * wsv[h][1]) + (xf[2] * wsv[h][2] + xf[3] * wsv[h][3]);
#pragma unroll
                        for (int o = 32; o > 0; o >>= 1) dsum += __shfl_xor(dsum, o, 64);
                        float ev = dsum + sdn[h];
                        ev = ev > 0.f ? ev : 0.2f * ev;
                        const float p = bf16_round(__expf(ev));
                        den[h] += p;
#pragma unroll
                        for (int i = 0; i < 4; ++i) acc[h][i] += p * xf[i];
                    }
                }
            }
#pragma unroll
            for (int h = 0; h < 4; ++h) {
                const float sc = 0.25f / (den[h] + 1e-9f);
                uint2 o;
                o.x = pack_bf16(acc[h][0] * sc, acc[h][1] * sc);
                o.y = pack_bf16(acc[h][2] * sc, acc[h][3] * sc);
                *(uint2*)(smem + L_Z + zaddr(nl, h * 32 + 4 * (lane >> 3) + (lane & 3)) + ((lane >> 2) & 1) * 8) = o;
            }
        }
    }
    asm volatile("s_waitcnt vmcnt(0)" ::: "memory");
    STAMP_AT(5);
    if (wv >= 8) {
        LDS_BARRIER();
        STAMP_AT(6); STAMP_AT(7); STAMP_AT(8); STAMP_RT(15); STAMP_FLUSH(ws, 1);
        return;
    }
    const u32x4* wt = (const u32x4*)(ws + WS_WT) + (size_t)wv * 64 * 64 + lane;
    u32x4 wr[WIN];
#pragma unroll
    for (int i = 0; i < WIN; ++i) wr[i] = wt[(NLF + i) * 64];
    __builtin_amdgcn_sched_barrier(0);
    LDS_BARRIER();
    STAMP_AT(6);
    {
        f32x4 acc[2][3];
#pragma unroll
        for (int t = 0; t < 2; ++t)
#pragma unroll
            for (int nt = 0; nt < 3; ++nt) acc[t][nt] = f32x4{0.f, 0.f, 0.f, 0.f};
        u32x4 zf[2][3];
#pragma unroll
        for (int nt = 0; nt < 3; ++nt) zf[0][nt] = *(const u32x4*)(smem + L_Z + zaddr(nt * 16 + c16, g));
#pragma unroll
        for (int ks = 0; ks < 32; ++ks) {
            if (ks + 1 < 32) {
#pragma unroll
                for (int nt = 0; nt < 3; ++nt)
                    zf[(ks + 1) & 1][nt] = *(const u32x4*)(smem + L_Z + zaddr(nt * 16 + c16, 4 * (ks + 1) + g));
            }
            u32x4 w0, w1;
            if (2 * ks < NLF) {
                w0 = *(const u32x4*)(smem + L_W + (wv * NLF + 2 * ks) * 1024 + lane * 16);
                w1 = *(const u32x4*)(smem + L_W + (wv * NLF + 2 * ks + 1) * 1024 + lane * 16);
            } else {
                w0 = wr[(ks * 2 - NLF) % WIN]; w1 = wr[(ks * 2 + 1 - NLF) % WIN];
            }
#pragma unroll
            for (int nt = 0; nt < 3; ++nt) {
                acc[0][nt] = mfma16(w0, zf[ks & 1][nt], acc[0][nt]);
                acc[1][nt] = mfma16(w1, zf[ks & 1][nt], acc[1][nt]);
            }
            if (2 * ks >= NLF && ks * 2 + WIN < 64) {
                wr[(ks * 2 - NLF) % WIN] = wt[(ks * 2 + WIN) * 64];
                wr[(ks * 2 + 1 - NLF) % WIN] = wt[(ks * 2 + 1 + WIN) * 64];
            }
            __builtin_amdgcn_sched_barrier(0);
        }
        STAMP_AT(7);
        asm volatile("gat_k2_tail_label:");
#pragma unroll
        for (int nt = 0; nt < 3; ++nt) {
            const int nl = nt * 16 + c16;
            if (nl < T) {
                u32x4 o;
                o[0] = pack_bf16(acc[0][nt][0], acc[0][nt][1]);
                o[1] = pack_bf16(acc[0][nt][2], acc[0][nt][3]);
                o[2] = pack_bf16(acc[1][nt][0], acc[1][nt][1]);
                o[3] = pack_bf16(acc[1][nt][2], acc[1][nt][3]);
                *(u32x4*)(out + (size_t)(n0 + nl) * DD + 32 * wv + 8 * g) = o;
            }
        }
    }
    STAMP_AT(8); STAMP_RT(15); STAMP_FLUSH(ws, 1);
}

extern "C" void kernel_launch(void* const* d_in, const int* in_sizes, int n_in, void* d_out, int out_size,
                              void* d_ws, size_t ws_size, hipStream_t stream) {
    const unsigned short* x = (const unsigned short*)d_in[0];
    const int* src = (const int*)d_in[1];
    const int* dst = (const int*)d_in[2];
    const unsigned short* w = (const unsigned short*)d_in[3];
    const unsigned short* a_src = (const unsigned short*)d_in[4];
    const unsigned short* a_dst = (const unsigned short*)d_in[5];
    (void)hipFuncSetAttribute((const void*)gat_k2, hipFuncAttributeMaxDynamicSharedMemorySize, L_TOTAL);
    gat_k1<<<256, 512, K1L_TOTAL, stream>>>(x, src, dst, w, a_src, a_dst, (char*)d_ws);
    gat_k2<<<NT, 640, L_TOTAL, stream>>>(x, src, dst, (const char*)d_ws, (unsigned short*)d_out);
}
```

```cpp
#define QPRIO
#include <hip/hip_runtime.h>
#include <hip/hip_bf16.h>
#include <stdint.h>

typedef __attribute__((ext_vector_type(8))) __bf16 bf16x8;
typedef __attribute__((ext_vector_type(2))) __bf16 bf16x2;
typedef __attribute__((ext_vector_type(4))) short s16x4;
typedef __attribute__((ext_vector_type(4))) float f32x4;
typedef __attribute__((ext_vector_type(2))) float f32x2;
typedef __attribute__((ext_vector_type(4))) unsigned u32x4;
typedef __attribute__((ext_vector_type(2))) unsigned u32x2;
typedef __attribute__((ext_vector_type(2))) int i32x2;
#define LAS __attribute__((address_space(3)))

constexpr int NN = 10000, NE = 160000, DD = 256, HH = 4;
constexpr int T = 40, NT = NN / T;
constexpr int NB = 64, CAP = 32, EPB = NE / NB;
constexpr int NG = T / 4;
constexpr int MAXQ = 58;

constexpr size_t WS_CELLS = 0;
constexpr size_t WS_WT    = 2097152;
constexpr size_t WS_WA    = WS_WT + 524288;
constexpr size_t WS_CTAB  = WS_WA + 8192;

__device__ __forceinline__ unsigned pack_bf16(float a, float b) {
    f32x2 v = {a, b};
    bf16x2 r = __builtin_convertvector(v, bf16x2);
    return __builtin_bit_cast(unsigned, r);
}
__device__ __forceinline__ float bf16_round(float a) {
    __bf16 r = (__bf16)a;
    return (float)r;
}
__device__ __forceinline__ f32x4 mfma16(u32x4 a, u32x4 b, f32x4 c) {
    return __builtin_amdgcn_mfma_f32_16x16x32_bf16(__builtin_bit_cast(bf16x8, a), __builtin_bit_cast(bf16x8, b), c, 0, 0, 0);
}
__device__ __forceinline__ f32x4 mfma16h(s16x4 a, u32x2 b, f32x4 c) {
    return __builtin_amdgcn_mfma_f32_16x16x16bf16_1k(a, __builtin_bit_cast(s16x4, b), c, 0, 0, 0);
}
__device__ __forceinline__ unsigned perm_lo(unsigned a, unsigned b) { return __builtin_amdgcn_perm(b, a, 0x05040100u); }
__device__ __forceinline__ unsigned perm_hi(unsigned a, unsigned b) { return __builtin_amdgcn_perm(b, a, 0x07060302u); }
__device__ __forceinline__ unsigned perm_sel(unsigned a, unsigned b, int hi) { return hi ? perm_hi(a, b) : perm_lo(a, b); }
__device__ __forceinline__ unsigned wave_incl_scan(unsigned v) {
    int x = (int)v;
    x += __builtin_amdgcn_update_dpp(0, x, 0x111, 0xF, 0xF, true);
    x += __builtin_amdgcn_update_dpp(0, x, 0x112, 0xF, 0xF, true);
    x += __builtin_amdgcn_update_dpp(0, x, 0x114, 0xF, 0xF, true);
    x += __builtin_amdgcn_update_dpp(0, x, 0x118, 0xF, 0xF, true);
    x += __builtin_amdgcn_update_dpp(0, x, 0x142, 0xA, 0xF, false);
    x += __builtin_amdgcn_update_dpp(0, x, 0x143, 0xC, 0xF, false);
    return (unsigned)x;
}

#define STAMP_DECL
#define STAMP_AT(idx)
#define STAMP_RT(idx)
#define STAMP_ACC(idx)
#define STAMP_FLUSH(wsp, kid)

#define LDS_BARRIER() do { asm volatile("s_waitcnt lgkmcnt(0)" ::: "memory"); __builtin_amdgcn_s_barrier(); asm volatile("" ::: "memory"); } while (0)
__device__ __forceinline__ void dma4(const void* gsrc, void* lds_wave_base) {
    __builtin_amdgcn_global_load_lds((const unsigned*)gsrc, (LAS unsigned*)lds_wave_base, 4, 0, 0);
}
__device__ __forceinline__ void dma16(const void* gsrc, void* lds_wave_base) {
    __builtin_amdgcn_global_load_lds((const unsigned*)gsrc, (LAS unsigned*)lds_wave_base, 16, 0, 0);
}
__device__ __forceinline__ unsigned lds_addr(const void* p) { return (unsigned)(uintptr_t)(LAS const char*)p; }

constexpr int K1L_ST = 0;
constexpr int K1L_TC = NT * CAP * 4 + 256;
constexpr int K1L_DUMP = K1L_TC + 2048;
constexpr int K1L_TOTAL = K1L_DUMP + 256;
#define K1COV 512

__global__ __launch_bounds__(512, 2) void gat_k1(const unsigned short* __restrict__ x, const int* __restrict__ src,
                                                 const int* __restrict__ dst, const unsigned short* __restrict__ w,
                                                 const unsigned short* __restrict__ a_src,
                                                 const unsigned short* __restrict__ a_dst, char* __restrict__ ws) {
    extern __shared__ __attribute__((aligned(16))) char smem[];
    const int tid = threadIdx.x, lane = tid & 63;
    const int wv = __builtin_amdgcn_readfirstlane(tid >> 6);
    const int b = blockIdx.x;
    STAMP_DECL STAMP_RT(14); STAMP_AT(0);
    if (b >= NB) {
        if (b < NB + 64 && wv == 0) {
            const int task = b - NB, h = task >> 4, rb = task & 15;
            const int m = lane & 15, kc = lane >> 4;
            char* lw = smem;
            char* la = smem + 8192;
            dma16((lane < 32 ? a_src : a_dst) + h * DD + (lane & 31) * 8, la);
            const unsigned short* wbase = w + ((size_t)h * DD + 16 * rb) * DD;
#pragma unroll
            for (int i = 0; i < 8; ++i) {
                const int row = 2 * i + (lane >> 5);
                dma16(wbase + row * DD + (((lane & 31) ^ row) * 8), lw + i * 1024);
            }
            asm volatile("s_waitcnt vmcnt(0)" ::: "memory");
            STAMP_AT(1);
            f32x4 acc0 = {0.f, 0.f, 0.f, 0.f}, acc1 = {0.f, 0.f, 0.f, 0.f};
#pragma unroll
            for (int ks = 0; ks < 8; ks += 2) {
                const u32x4 b0 = *(const u32x4*)(la + (m & 1) * 512 + (4 * ks + kc) * 16);
                const u32x4 b1 = *(const u32x4*)(la + (m & 1) * 512 + (4 * ks + 4 + kc) * 16);
                const u32x4 a0 = *(const u32x4*)(lw + m * 512 + (((4 * ks + kc) ^ m) * 16));
                const u32x4 a1 = *(const u32x4*)(lw + m * 512 + (((4 * ks + 4 + kc) ^ m) * 16));
                acc0 = mfma16(a0, b0, acc0);
                acc1 = mfma16(a1, b1, acc1);
            }
            acc0 += acc1;
            u32x2 hi, lo;
            hi[0] = pack_bf16(acc0[0], acc0[1]); hi[1] = pack_bf16(acc0[2], acc0[3]);
            lo[0] = pack_bf16(acc0[0] - __uint_as_float(hi[0] << 16), acc0[1] - __uint_as_float(hi[0] & 0xFFFF0000u));
            lo[1] = pack_bf16(acc0[2] - __uint_as_float(hi[1] << 16), acc0[3] - __uint_as_float(hi[1] & 0xFFFF0000u));
            if (m < 4) {
                const int d = 16 * rb + 4 * kc, cd = d >> 3;
                const int ks = cd & 7, kq = ((cd >> 4) & 1) | (((cd >> 3) & 1) << 1);
                const int mr = 4 * h + 2 * (m & 1) + (m >> 1);
                *(u32x2*)(ws + WS_WA + ks * 1024 + (kq * 16 + mr) * 16 + (d & 7) * 2) = (m >> 1) ? lo : hi;
            }
            STAMP_AT(2);
        } else if (b >= NB + 64 && b < NB + 80 && wv < 3) {
            unsigned long long pc; int off, len;
            asm volatile("s_getpc_b64 %0\n.Lpc%=:\n\ts_mov_b32 %1, gat_k2_head_label-.Lpc%=\n\ts_mov_b32 %2, gat_k2_tail_label-gat_k2_head_label"
                         : "=s"(pc), "=s"(off), "=s"(len));
            if (tid * 128 < len + 128 && len < 65536) dma4((const char*)(((pc + off) & ~127ull) + tid * 128), smem + K1L_DUMP);
        }
        if (tid < K1COV) {
            unsigned line = (unsigned)(b & 7) * 5000u + ((unsigned)(b - NB) >> 3) * K1COV + (unsigned)tid;
            line = line >= 40000u ? line - 40000u : line;
            dma4((const char*)x + (size_t)line * 128, smem + K1L_DUMP);
        }
    } else {
        const int c = b;
        unsigned* stage = (unsigned*)(smem + K1L_ST);
        unsigned long long* tcnt = (unsigned long long*)(smem + K1L_TC);
        if (wv < 7) {
            const i32x2* d2 = (const i32x2*)(dst + c * EPB);
            const i32x2* s2 = (const i32x2*)(src + c * EPB);
            i32x2 dv[3], sv[3];
#pragma unroll
            for (int r = 0; r < 3; ++r) {
                const int i = tid + 448 * r;
                const int ii = i < EPB / 2 ? i : 0;
                dv[r] = d2[ii]; sv[r] = s2[ii];
            }
            __builtin_amdgcn_sched_barrier(0);
            if (tid < 256) tcnt[tid] = 0ull;
            LDS_BARRIER();
            __builtin_amdgcn_sched_barrier(0);
            STAMP_AT(1);
            unsigned long long old[6]; unsigned tt[6], dl[6];
#pragma unroll
            for (int k = 0; k < 6; ++k) {
                const unsigned d = (unsigned)dv[k >> 1][k & 1];
                tt[k] = d / (unsigned)T;
                dl[k] = d - tt[k] * T;
            }
#pragma unroll
            for (int k = 0; k < 4; ++k) old[k] = atomicAdd(&tcnt[tt[k]], (1ull << 50) | (1ull << (5 * (dl[k] >> 2))));
            old[4] = old[5] = ~0ull;
            if (tid + 896 < EPB / 2) {
#pragma unroll
                for (int k = 4; k < 6; ++k) old[k] = atomicAdd(&tcnt[tt[k]], (1ull << 50) | (1ull << (5 * (dl[k] >> 2))));
            }
            unsigned ent[6];
#pragma unroll
            for (int k = 0; k < 6; ++k) {
                const unsigned grp = dl[k] >> 2;
                const unsigned j = (grp * 11u) >> 5, sh = (grp - 3u * j) * 10u;
                ent[k] = (unsigned)sv[k >> 1][k & 1] | (dl[k] << 14) | (j << 25) | (sh << 27);
            }
            __builtin_amdgcn_sched_barrier(0);
#pragma unroll
            for (int k = 0; k < 6; ++k) {
                const unsigned pos = (unsigned)(old[k] >> 50);
                const unsigned rank = (unsigned)(old[k] >> (5 * (dl[k] >> 2))) & 31u;
                const unsigned idx = pos < (unsigned)CAP ? tt[k] * CAP + pos : (unsigned)(NT * CAP + lane);
                stage[idx] = ent[k] | (rank << 20);
            }
        } else {
            const int ks = c >> 1;
            const int kc = 2 * (c & 1) + (lane >> 5), fc = lane & 31;
            u32x4 r[8];
#pragma unroll
            for (int j = 0; j < 8; ++j) r[j] = *(const u32x4*)(w + (size_t)(32 * ks + 16 * (j >> 2) + 4 * kc + (j & 3)) * DD + 8 * fc);
            __builtin_amdgcn_sched_barrier(0);
            __builtin_amdgcn_s_barrier();
            __builtin_amdgcn_sched_barrier(0);
            const int wq = fc >> 2, gp = fc & 3;
            char* wt = ws + WS_WT + (size_t)((wq * 32 + ks) * 2) * 1024 + (kc * 16 + 4 * gp) * 16;
#pragma unroll
            for (int i = 0; i < 8; ++i) {
                u32x4 o;
#pragma unroll
                for (int p = 0; p < 4; ++p) o[p] = perm_sel(r[2 * p][i >> 1], r[2 * p + 1][i >> 1], i & 1);
                *(u32x4*)(wt + (i >> 2) * 1024 + (i & 3) * 16) = o;
            }
        }
        LDS_BARRIER();
        STAMP_AT(2);
        u32x4* cells4 = (u32x4*)(ws + WS_CELLS);
        u32x4 v[4]; unsigned tc4[4];
#pragma unroll
        for (int i = 0; i < 4; ++i) {
            const int ii = tid + 512 * i < NT * CAP / 4 ? tid + 512 * i : 0;
            v[i] = ((u32x4*)stage)[ii];
            tc4[i] = ((const unsigned*)tcnt)[(ii >> 3) * 2 + 1] >> 18;
        }
        const unsigned long long cw = tcnt[tid & 255];
#pragma unroll
        for (int i = 0; i < 4; ++i) {
            const int ii = tid + 512 * i;
            if (ii < NT * CAP / 4 && (unsigned)((ii & 7) * 4) < tc4[i]) cells4[((ii >> 3) * NB + c) * 8 + (ii & 7)] = v[i];
        }
        if (tid < NT) {
            unsigned tc = (unsigned)(cw >> 50);
            const unsigned bad = (tc > 31u || (cw & 0x2108421084210ull) != 0ull) ? 1u : 0u;
            tc = tc > 31u ? 31u : tc;
            unsigned gc[NG];
#pragma unroll
            for (int k = 0; k < NG; ++k) gc[k] = (unsigned)(cw >> (5 * k)) & 15u;
            u32x4 o;
            o[0] = gc[0] | (gc[1] << 10) | (gc[2] << 20);
            o[1] = gc[3] | (gc[4] << 10) | (gc[5] << 20);
            o[2] = gc[6] | (gc[7] << 10) | (gc[8] << 20);
            o[3] = gc[9] | (tc << 10) | (bad << 24);
            ((u32x4*)(ws + WS_CTAB))[tid * NB + c] = o;
        }
    }
    asm volatile("s_waitcnt vmcnt(0)" ::: "memory");
    STAMP_AT(3); STAMP_RT(15); STAMP_FLUSH(ws, 0);
}

constexpr int NLF = 6;
constexpr int L_Z = 0;
constexpr int L_W = L_Z + T * 2048;
constexpr int L_WA = L_W + 8 * NLF * 1024;
constexpr int L_P = L_WA + 8192;
constexpr int L_EL = L_P + NG * 2048;
constexpr int L_TOTAL = L_EL + (MAXQ + 2) * 64 + 256;
static_assert(L_TOTAL <= 163840, "LDS budget");
constexpr unsigned OOB = 0x80000000u;
#define WIN 24

__device__ __forceinline__ int zaddr(int node, int ch) {
    return node * 2048 + 16 * (ch ^ ((node ^ (2 * (ch >> 5))) & 15));
}
__device__ __forceinline__ unsigned sw2(unsigned r) { return ((r & 7u) << 1) | (r >> 3); }

__global__ __launch_bounds__(640, 3) void gat_k2(const unsigned short* __restrict__ x, const int* __restrict__ src,
                                                 const int* __restrict__ dst, const char* __restrict__ ws,
                                                 unsigned short* __restrict__ out) {
    extern __shared__ __attribute__((aligned(16))) char smem[];
    asm volatile("gat_k2_head_label:");
    const int tid = threadIdx.x, lane = tid & 63;
    const int wv = __builtin_amdgcn_readfirstlane(tid >> 6);
    const int tile = blockIdx.x;
    const int n0 = tile * T;
    unsigned* elist = (unsigned*)(smem + L_EL);
    const int c16 = lane & 15, g = lane >> 4;
    char* gbuf = smem + L_Z + wv * 8192;
    const unsigned gbuf_a = lds_addr(smem) + L_Z + wv * 8192;
#define ABL_X (NN * DD * 2 + 4096)
    const uintptr_t xbase = (uintptr_t)x - 4096;
    const u32x4 xr = {(unsigned)xbase, (unsigned)(xbase >> 32) & 0xFFFFu, (unsigned)(ABL_X), 0x00020000u};
    const unsigned lch = (unsigned)(lane & 31) * 16u;

    STAMP_DECL STAMP_RT(14); STAMP_AT(0);
#define DMA2(gp, la) do { unsigned keep_; \
        asm volatile("s_mov_b32 %0, m0\n\ts_mov_b32 m0, %2\n\ts_nop 0\n\t" \
                     "global_load_lds_dwordx4 %1, off\n\tglobal_load_lds_dwordx4 %1, off offset:1024\n\t" \
                     "s_mov_b32 m0, %0" : "=&s"(keep_) : "v"(gp), "s"(la) : "memory"); } while (0)
    u32x4 ct, ce;
    const unsigned own_a = lds_addr(smem) + L_P + wv * 2048;
    {
        const u32x4* pct = (const u32x4*)(ws + WS_CTAB) + tile * NB + lane;
        const u32x4* pce = (const u32x4*)(ws + WS_CELLS) + tile * (NB * CAP / 4) + (tid & 511);
        asm volatile("global_load_dwordx4 %0, %2, off\n\tglobal_load_dwordx4 %1, %3, off"
                     : "=&v"(ct), "=&v"(ce) : "v"(pct), "v"(pce) : "memory");
    }
    if (wv >= 8) {
        const char* wap = ws + WS_WA + (wv - 8) * 4096 + lane * 16;
        const unsigned wal = lds_addr(smem) + L_WA + (wv - 8) * 4096;
        DMA2(wap, wal); DMA2(wap + 2048, wal + 2048);
    }
    {
        const u32x4 o4 = {OOB, OOB, OOB, OOB}, z4 = {0u, 0u, 0u, 0u};
        if (tid < (MAXQ + 2) * 4) ((u32x4*)elist)[tid] = o4;
#pragma unroll
        for (int i = 0; i < 8; ++i) *(u32x4*)(gbuf + i * 1024 + lane * 16) = z4;
    }
    LDS_BARRIER();
    {
        const unsigned r0 = (unsigned)(lane >> 5), r1 = r0 + 2u;
        const unsigned o0 = ((unsigned)(n0 + 4 * wv) + r0) * 512u + 4096u + (lch ^ (sw2(r0) << 4));
        const unsigned o1 = ((unsigned)(n0 + 4 * wv) + r1) * 512u + 3072u + (lch ^ (sw2(r1) << 4));
        unsigned keep;
        asm volatile("s_mov_b32 %0, m0\n\ts_mov_b32 m0, %4\n\ts_nop 0\n\t"
                     "buffer_load_dwordx4 %1, %3, 0 offen lds\n\tbuffer_load_dwordx4 %2, %3, 0 offen offset:1024 lds\n\t"
                     "s_mov_b32 m0, %0"
                     : "=&s"(keep) : "v"(o0), "v"(o1), "s"(xr), "s"(own_a) : "memory");
    }
    if (wv >= 8) {
        const char* wsrc = ws + WS_WT + (size_t)(wv - 8) * 4 * 65536 + lane * 16;
        const unsigned wl = lds_addr(smem) + L_W + (wv - 8) * 4 * NLF * 1024;
#pragma unroll
        for (int i = 0; i < 4 * NLF; i += 2) DMA2(wsrc + (i / NLF) * 65536 + (i % NLF) * 1024, wl + i * 1024);
    }
    asm volatile("s_cmp_lt_i32 %2, 8\n\ts_cbranch_scc1 .Lwa%=\n\ts_waitcnt vmcnt(%3)\n\ts_branch .Lwb%=\n"
                 ".Lwa%=:\n\ts_waitcnt vmcnt(2)\n.Lwb%=:"
                 : "+v"(ct), "+v"(ce) : "s"(wv), "i"(4 * NLF + 2) : "scc", "memory");
    u32x4 inc;
#pragma unroll
    for (int k = 0; k < 4; ++k) inc[k] = wave_incl_scan(ct[k]);
    unsigned tot[4];
#pragma unroll
    for (int k = 0; k < 4; ++k) tot[k] = (unsigned)__builtin_amdgcn_readlane((int)inc[k], 63);
    const bool slow = (tot[3] >> 24) != 0u || ((tot[3] >> 10) & 0x3FFFu) > (unsigned)(MAXQ * 16 - 15 * NG);
    unsigned base[4];
    {
        unsigned run = 0;
#pragma unroll
        for (int k = 0; k < 4; ++k) {
            const unsigned pad = ((tot[k] & (k < 3 ? 0x3FFFFFFFu : 0x3FFu)) + 0x00F03C0Fu) & 0x3F0FC3F0u;
            const unsigned incl = pad * 0x00100401u;
            base[k] = incl - pad + run * 0x00100401u;
            run += (incl >> 20) & 1023u;
        }
    }
    int qi, left;
    {
        const int jw = (wv * 11) >> 5, sh = (wv - 3 * jw) * 10;
        const unsigned bw = jw == 0 ? base[0] : (jw == 1 ? base[1] : (jw == 2 ? base[2] : base[3]));
        const unsigned tw = jw == 0 ? tot[0] : (jw == 1 ? tot[1] : (jw == 2 ? tot[2] : tot[3]));
        qi = (int)(((bw >> sh) & 1023u) >> 4);
        left = (int)((tw >> sh) & 1023u);
    }
    STAMP_AT(1);
    if (wv < 8) {
        unsigned pre0, pre1, pre2, pre3, ccnt;
        {
            unsigned ex[4];
#pragma unroll
            for (int k = 0; k < 4; ++k) ex[k] = inc[k] - ct[k] + base[k];
            ex[3] = (ex[3] & 1023u) | (ct[3] & (63u << 10));
            const int sl = (wv * 8 + (lane >> 3)) * 4;
            pre0 = (unsigned)__builtin_amdgcn_ds_bpermute(sl, (int)ex[0]);
            pre1 = (unsigned)__builtin_amdgcn_ds_bpermute(sl, (int)ex[1]);
            pre2 = (unsigned)__builtin_amdgcn_ds_bpermute(sl, (int)ex[2]);
            pre3 = (unsigned)__builtin_amdgcn_ds_bpermute(sl, (int)ex[3]);
            asm volatile("" : "+v"(pre0), "+v"(pre1), "+v"(pre2), "+v"(pre3));
            ccnt = slow ? 0u : (pre3 >> 10);
        }
#pragma unroll
        for (int k = 0; k < 4; ++k) {
            const unsigned e = ce[k];
            const bool j0 = (e >> 25) & 1u, j1 = (e >> 26) & 1u;
            const unsigned lo2 = j0 ? pre1 : pre0, hi2 = j0 ? pre3 : pre2;
            const unsigned dw = j1 ? hi2 : lo2;
            const unsigned slot = ((dw >> (e >> 27)) & 1023u) + ((e >> 20) & 31u);
            const unsigned r = slot & 15u;
            unsigned pos = (slot & ~15u) | ((r & 1u) << 3) | (r >> 1);
            pos = ((unsigned)((lane & 7) * 4 + k) < ccnt) ? pos : (unsigned)((MAXQ + 2) * 16 + lane);
            elist[pos] = (((e & 0x3FFFu) << 9) + 4096u - ((r & 6u) << 9)) | (sw2(r) << 4) | ((e >> 14) & 3u);
        }
    }
    unsigned rowa = gbuf_a + c16 * 512 + (g & 1) * 256 + ((sw2((unsigned)c16) ^ ((unsigned)(g >> 1) << 3)) << 4);
    const char* wap = smem + L_WA + lane * 16;
#define READ_ROWS() do { asm volatile("" : "+v"(rowa)); \
        _Pragma("unroll") for (int ks = 0; ks < 8; ++ks) xb[ks] = *(const LAS u32x4*)(uintptr_t)(rowa ^ ((unsigned)ks << 4)); } while (0)
#define WAF(k) (*(const u32x4*)(wap + (k) * 1024))
#define LOGITS(LG) do { f32x4 l0 = {0.f, 0.f, 0.f, 0.f}, l1 = {0.f, 0.f, 0.f, 0.f}; \
        _Pragma("unroll") for (int ks = 0; ks < 8; ks += 2) { l0 = mfma16(wa[ks], xb[ks], l0); l1 = mfma16(wa[ks + 1], xb[ks + 1], l1); } \
        LG = l0 + l1; } while (0)
    u32x4 wa[8];
    u32x4 xb[8];
    LDS_BARRIER();
    STAMP_AT(2);
    if (left > 80) __builtin_amdgcn_s_setprio(3); else if (left > 64) __builtin_amdgcn_s_setprio(2); else __builtin_amdgcn_s_setprio(1);
    if (slow) left = 0;
    const int epos = ((c16 & 1) << 3) | (c16 >> 1);
    unsigned fe, ao[8];
#define PREP() do { \
        const u32x4 e0 = *(const u32x4*)(elist + qi * 16 + (lane >> 5) * 8), e1 = *(const u32x4*)(elist + qi * 16 + (lane >> 5) * 8 + 4); \
        fe = elist[qi * 16 + epos]; \
        _Pragma("unroll") for (int k = 0; k < 4; ++k) { ao[k] = (e0[k] & ~15u) ^ lch; ao[4 + k] = (e1[k] & ~15u) ^ lch; } } while (0)
#define ISSUE() do { \
        unsigned keep; \
        asm volatile("s_waitcnt lgkmcnt(0)\n\ts_mov_b32 %0, m0\n\ts_mov_b32 m0, %10\n\t" \
                     "s_cmp_lt_i32 %11, 15\n\ts_cbranch_scc1 .Lp%=\n\t" \
                     "buffer_load_dwordx4 %1, %9, 0 offen lds\n\tbuffer_load_dwordx4 %2, %9, 0 offen offset:1024 lds\n\t" \
                     "buffer_load_dwordx4 %3, %9, 0 offen offset:2048 lds\n\tbuffer_load_dwordx4 %4, %9, 0 offen offset:3072 lds\n\t" \
                     "s_add_u32 m0, m0, 0x1000\n\ts_nop 0\n\t" \
                     "buffer_load_dwordx4 %5, %9, 0 offen lds\n\tbuffer_load_dwordx4 %6, %9, 0 offen offset:1024 lds\n\t" \
                     "buffer_load_dwordx4 %7, %9, 0 offen offset:2048 lds\n\tbuffer_load_dwordx4 %8, %9, 0 offen offset:3072 lds\n\t" \
                     "s_branch .Lq%=\n.Lp%=:\n\t" \
                     "s_cmp_lt_i32 %11, 1\n\ts_cbranch_scc1 .Lq%=\n\tbuffer_load_dwordx4 %1, %9, 0 offen lds\n\t" \
                     "s_cmp_lt_i32 %11, 3\n\ts_cbranch_scc1 .Lq%=\n\tbuffer_load_dwordx4 %2, %9, 0 offen offset:1024 lds\n\t" \
                     "s_cmp_lt_i32 %11, 5\n\ts_cbranch_scc1 .Lq%=\n\tbuffer_load_dwordx4 %3, %9, 0 offen offset:2048 lds\n\t" \
                     "s_cmp_lt_i32 %11, 7\n\ts_cbranch_scc1 .Lq%=\n\tbuffer_load_dwordx4 %4, %9, 0 offen offset:3072 lds\n\t" \
                     "s_add_u32 m0, m0, 0x1000\n\t" \
                     "s_cmp_lt_i32 %11, 9\n\ts_cbranch_scc1 .Lq%=\n\tbuffer_load_dwordx4 %5, %9, 0 offen lds\n\t" \
                     "s_cmp_lt_i32 %11, 11\n\ts_cbranch_scc1 .Lq%=\n\tbuffer_load_dwordx4 %6, %9, 0 offen offset:1024 lds\n\t" \
                     "s_cmp_lt_i32 %11, 13\n\ts_cbranch_scc1 .Lq%=\n\tbuffer_load_dwordx4 %7, %9, 0 offen offset:2048 lds\n" \
                     ".Lq%=:\n\ts_mov_b32 m0, %0" \
                     : "=&s"(keep) \
                     : "v"(ao[0]), "v"(ao[1]), "v"(ao[2]), "v"(ao[3]), "v"(ao[4]), "v"(ao[5]), "v"(ao[6]), "v"(ao[7]), \
                       "s"(xr), "s"(gbuf_a), "s"(left) : "scc", "memory"); } while (0)
    PREP();
    asm volatile("s_cmp_lt_i32 %0, 8\n\ts_cbranch_scc1 .Lwa%=\n\ts_waitcnt vmcnt(%1)\n\ts_branch .Lwb%=\n"
                 ".Lwa%=:\n\ts_waitcnt vmcnt(0)\n.Lwb%=:" :: "s"(wv), "i"(4 * NLF) : "scc", "memory");
    ISSUE();
    float sdq;
    {
        const unsigned oa = own_a + (c16 & 3) * 512 + (g & 1) * 256 + ((sw2((unsigned)(c16 & 3)) ^ ((unsigned)(g >> 1) << 3)) << 4);
#pragma unroll
        for (int ks = 0; ks < 8; ++ks) xb[ks] = *(const LAS u32x4*)(uintptr_t)(oa ^ ((unsigned)ks << 4));
#pragma unroll
        for (int k = 0; k < 8; ++k) wa[k] = WAF(k);
        f32x4 lg; LOGITS(lg); sdq = lg[2] + lg[3];
    }
    STAMP_AT(3); STAMP_AT(4); STAMP_AT(13);

    if (!slow) {
        const s16x4 ones2 = {0x3F80, 0x3F80, 0x3F80, 0x3F80};
        char* pscr = smem + L_P + wv * 2048;
        f32x4 acc[16], accd = {0.f, 0.f, 0.f, 0.f};
#pragma unroll
        for (int i = 0; i < 16; ++i) acc[i] = f32x4{0.f, 0.f, 0.f, 0.f};
        const unsigned trow = 4u * g + ((unsigned)c16 >> 2), tp = (unsigned)c16 & 3u;
        unsigned tra = gbuf_a + trow * 512 + (tp & 1u) * 8 + ((sw2(trow) ^ (tp >> 1)) << 4);
        *(u32x2*)(pscr + lane * 8) = u32x2{0u, 0u};
#pragma unroll 1
        while (left > 0) {
            left -= 16; qi += 1;
            const unsigned fc = fe;
            const float sdl = __builtin_bit_cast(float, __builtin_amdgcn_ds_bpermute((int)((g * 16 + (fc & 3u)) * 4), __builtin_bit_cast(int, sdq)));
            asm volatile("s_waitcnt vmcnt(0)" ::: "memory");
            asm volatile("" : "+v"(rowa));
            f32x4 l0 = {0.f, 0.f, 0.f, 0.f}, l1 = {0.f, 0.f, 0.f, 0.f};
#define XR(ks) (*(const LAS u32x4*)(uintptr_t)(rowa ^ ((unsigned)(ks) << 4)))
            {
                const u32x4 x0 = XR(0), x1 = XR(1), x2 = XR(2), x3 = XR(3), x4 = XR(4), x5 = XR(5);
                __builtin_amdgcn_sched_barrier(0);
                l0 = mfma16(wa[0], x0, l0); l1 = mfma16(wa[1], x1, l1);
                l0 = mfma16(wa[2], x2, l0); l1 = mfma16(wa[3], x3, l1);
                __builtin_amdgcn_sched_barrier(0);
                const u32x4 x6 = XR(6), x7 = XR(7);
                __builtin_amdgcn_sched_barrier(0);
                l0 = mfma16(wa[4], x4, l0); l1 = mfma16(wa[5], x5, l1);
                l0 = mfma16(wa[6], x6, l0); l1 = mfma16(wa[7], x7, l1);
                __builtin_amdgcn_sched_barrier(0);
            }
            s16x4 tf[16];
            asm volatile("" : "+v"(tra));
#pragma unroll
            for (int t = 0; t < 8; ++t) {
                const unsigned ta = tra ^ ((unsigned)t << 5);
                tf[t] = __builtin_amdgcn_ds_read_tr16_b64_v4i16((LAS s16x4*)(uintptr_t)ta);
                tf[t + 8] = __builtin_amdgcn_ds_read_tr16_b64_v4i16((LAS s16x4*)(uintptr_t)(ta + 256));
            }
            __builtin_amdgcn_sched_barrier(0);
            float e = ((l0[0] + l1[0]) + (l0[1] + l1[1])) + sdl;
            e = e > 0.f ? e : 0.2f * e;
            float p = __expf(e);
            p = fc != OOB ? p : 0.f;
            __builtin_amdgcn_sched_barrier(0);
            PREP();
            *(__bf16*)(pscr + ((c16 >> 2) * 16 + (fc & 3u) * 4 + g) * 8 + (c16 & 3) * 2) = (__bf16)p;
            const u32x2 pf = *(const u32x2*)(pscr + lane * 8);
            *(u32x2*)(pscr + lane * 8) = u32x2{0u, 0u};
            ISSUE();
#pragma unroll
            for (int t = 0; t < 16; ++t) acc[t] = mfma16h(tf[t], pf, acc[t]);
            accd = mfma16h(ones2, pf, accd);
        }
        {
            const float sc = 0.25f * __builtin_amdgcn_rcpf(accd[0] + 1e-9f);
            const int node = wv * 4 + (c16 >> 2), h = c16 & 3;
#pragma unroll
            for (int u = 0; u < 8; ++u) {
                const f32x4 a = acc[2 * u] * sc, b2 = acc[2 * u + 1] * sc;
                u32x4 o;
                o[0] = pack_bf16(a[0], a[1]); o[1] = pack_bf16(a[2], a[3]);
                o[2] = pack_bf16(b2[0], b2[1]); o[3] = pack_bf16(b2[2], b2[3]);
                *(u32x4*)(smem + L_Z + zaddr(node, h * 32 + 4 * u + g)) = o;
            }
        }
    } else {
        float wsv[4][4];
        {
            const int d = 4 * lane, cd = d >> 3;
            const int ks = cd & 7, kq = ((cd >> 4) & 1) | (((cd >> 3) & 1) << 1);
#pragma unroll
            for (int h = 0; h < 4; ++h) {
                const u32x2 hi = *(const u32x2*)(ws + WS_WA + ks * 1024 + (kq * 16 + 4 * h) * 16 + (d & 7) * 2);
                const u32x2 lo = *(const u32x2*)(ws + WS_WA + ks * 1024 + (kq * 16 + 4 * h + 1) * 16 + (d & 7) * 2);
                wsv[h][0] = __uint_as_float(hi[0] << 16) + __uint_as_float(lo[0] << 16);
                wsv[h][1] = __uint_as_float(hi[0] & 0xFFFF0000u) + __uint_as_float(lo[0] & 0xFFFF0000u);
                wsv[h][2] = __uint_as_float(hi[1] << 16) + __uint_as_float(lo[1] << 16);
                wsv[h][3] = __uint_as_float(hi[1] & 0xFFFF0000u) + __uint_as_float(lo[1] & 0xFFFF0000u);
            }
        }
#pragma unroll 1
        for (int nn = 0; nn < 4; ++nn) {
            const int nl = wv * 4 + nn;
            const int node = n0 + nl;
            float acc[4][4], den[4], sdn[4];
#pragma unroll
            for (int h = 0; h < 4; ++h) { den[h] = 0.f; sdn[h] = __shfl(sdq, h * 16 + nn, 64);
#pragma unroll
                for (int i = 0; i < 4; ++i) acc[h][i] = 0.f; }
#pragma unroll 1
            for (int e0 = 0; e0 < NE; e0 += 64) {
                const int e = e0 + lane;
                const int dv = dst[e];
                const int sv = src[e];
                unsigned long long mask = __ballot(dv == node);
                while (mask) {
                    const int bit = __builtin_ctzll(mask);
                    mask &= mask - 1;
                    const int s = __shfl(sv, bit, 64);
                    const uint2 xv = *(const uint2*)(x + (size_t)s * DD + lane * 4);
                    const float xf[4] = {__uint_as_float(xv.x << 16), __uint_as_float(xv.x & 0xFFFF0000u),
                                         __uint_as_float(xv.y << 16), __uint_as_float(xv.y & 0xFFFF0000u)};
#pragma unroll
                    for (int h = 0; h < 4; ++h) {
                        float dsum = (xf[0] * wsv[h][0] + xf[1] * wsv[h][1]) + (xf[2] * wsv[h][2] + xf[3] * wsv[h][3]);
#pragma unroll
                        for (int o = 32; o > 0; o >>= 1) dsum += __shfl_xor(dsum, o, 64);
                        float ev = dsum + sdn[h];
                        ev = ev > 0.f ? ev : 0.2f * ev;
                        const float p = bf16_round(__expf(ev));
                        den[h] += p;
#pragma unroll
                        for (int i = 0; i < 4; ++i) acc[h][i] += p * xf[i];
                    }
                }
            }
#pragma unroll
            for (int h = 0; h < 4; ++h) {
                const float sc = 0.25f / (den[h] + 1e-9f);
                uint2 o;
                o.x = pack_bf16(acc[h][0] * sc, acc[h][1] * sc);
                o.y = pack_bf16(acc[h][2] * sc, acc[h][3] * sc);
                *(uint2*)(smem + L_Z + zaddr(nl, h * 32 + 4 * (lane >> 3) + (lane & 3)) + ((lane >> 2) & 1) * 8) = o;
            }
        }
    }
    asm volatile("s_waitcnt vmcnt(0)" ::: "memory");
    __builtin_amdgcn_s_setprio(0);
    STAMP_AT(5);
    if (wv >= 8) {
        LDS_BARRIER();
        STAMP_AT(6); STAMP_AT(7); STAMP_AT(8); STAMP_RT(15); STAMP_FLUSH(ws, 1);
        return;
    }
    const u32x4* wt = (const u32x4*)(ws + WS_WT) + (size_t)wv * 64 * 64 + lane;
    u32x4 wr[WIN];
#pragma unroll
    for (int i = 0; i < WIN; ++i) wr[i] = wt[(NLF + i) * 64];
    __builtin_amdgcn_sched_barrier(0);
    LDS_BARRIER();
    STAMP_AT(6);
    {
        f32x4 acc[2][3];
#pragma unroll
        for (int t = 0; t < 2; ++t)
#pragma unroll
            for (int nt = 0; nt < 3; ++nt) acc[t][nt] = f32x4{0.f, 0.f, 0.f, 0.f};
        u32x4 zf[2][3];
#pragma unroll
        for (int nt = 0; nt < 3; ++nt) zf[0][nt] = *(const u32x4*)(smem + L_Z + zaddr(nt * 16 + c16, g));
#pragma unroll
        for (int ks = 0; ks < 32; ++ks) {
            if (ks + 1 < 32) {
#pragma unroll
                for (int nt = 0; nt < 3; ++nt)
                    zf[(ks + 1) & 1][nt] = *(const u32x4*)(smem + L_Z + zaddr(nt * 16 + c16, 4 * (ks + 1) + g));
            }
            u32x4 w0, w1;
            if (2 * ks < NLF) {
                w0 = *(const u32x4*)(smem + L_W + (wv * NLF + 2 * ks) * 1024 + lane * 16);
                w1 = *(const u32x4*)(smem + L_W + (wv * NLF + 2 * ks + 1) * 1024 + lane * 16);
            } else {
                w0 = wr[(ks * 2 - NLF) % WIN]; w1 = wr[(ks * 2 + 1 - NLF) % WIN];
            }
#pragma unroll
            for (int nt = 0; nt < 3; ++nt) {
                acc[0][nt] = mfma16(w0, zf[ks & 1][nt], acc[0][nt]);
                acc[1][nt] = mfma16(w1, zf[ks & 1][nt], acc[1][nt]);
            }
            if (2 * ks >= NLF && ks * 2 + WIN < 64) {
                wr[(ks * 2 - NLF) % WIN] = wt[(ks * 2 + WIN) * 64];
                wr[(ks * 2 + 1 - NLF) % WIN] = wt[(ks * 2 + 1 + WIN) * 64];
            }
            __builtin_amdgcn_sched_barrier(0);
        }
        STAMP_AT(7);
#pragma unroll
        for (int nt = 0; nt < 3; ++nt) {
            const int nl = nt * 16 + c16;
            if (nl < T) {
                u32x4 o;
                o[0] = pack_bf16(acc[0][nt][0], acc[0][nt][1]);
                o[1] = pack_bf16(acc[0][nt][2], acc[0][nt][3]);
                o[2] = pack_bf16(acc[1][nt][0], acc[1][nt][1]);
                o[3] = pack_bf16(acc[1][nt][2], acc[1][nt][3]);
                *(u32x4*)(out + (size_t)(n0 + nl) * DD + 32 * wv + 8 * g) = o;
            }
        }
    }
    STAMP_AT(8); STAMP_RT(15); STAMP_FLUSH(ws, 1);
    asm volatile("gat_k2_tail_label:");
}

extern "C" void kernel_launch(void* const* d_in, const int* in_sizes, int n_in, void* d_out, int out_size,
                              void* d_ws, size_t ws_size, hipStream_t stream) {
    const unsigned short* x = (const unsigned short*)d_in[0];
    const int* src = (const int*)d_in[1];
    const int* dst = (const int*)d_in[2];
    const unsigned short* w = (const unsigned short*)d_in[3];
    const unsigned short* a_src = (const unsigned short*)d_in[4];
    const unsigned short* a_dst = (const unsigned short*)d_in[5];
    (void)hipFuncSetAttribute((const void*)gat_k2, hipFuncAttributeMaxDynamicSharedMemorySize, L_TOTAL);
    gat_k1<<<256, 512, K1L_TOTAL, stream>>>(x, src, dst, w, a_src, a_dst, (char*)d_ws);
    gat_k2<<<NT, 640, L_TOTAL, stream>>>(x, src, dst, (const char*)d_ws, (unsigned short*)d_out);
}
```

```cpp
#include <hip/hip_runtime.h>
#include <hip/hip_bf16.h>
#include <stdint.h>

typedef __attribute__((ext_vector_type(8))) __bf16 bf16x8;
typedef __attribute__((ext_vector_type(2))) __bf16 bf16x2;
typedef __attribute__((ext_vector_type(4))) short s16x4;
typedef __attribute__((ext_vector_type(4))) float f32x4;
typedef __attribute__((ext_vector_type(2))) float f32x2;
typedef __attribute__((ext_vector_type(4))) unsigned u32x4;
typedef __attribute__((ext_vector_type(2))) unsigned u32x2;
typedef __attribute__((ext_vector_type(2))) int i32x2;
#define LAS __attribute__((address_space(3)))

constexpr int NN = 10000, NE = 160000, DD = 256, HH = 4;
constexpr int T = 40, NT = NN / T;
constexpr int NB = 64, CAP = 32, EPB = NE / NB;
constexpr int NG = T / 4;
constexpr int MAXQ = 58;

constexpr size_t WS_CELLS = 0;
constexpr size_t WS_WT    = 2097152;
constexpr size_t WS_WA    = WS_WT + 524288;
constexpr size_t WS_CTAB  = WS_WA + 8192;
constexpr size_t WS_CTL   = WS_CTAB + 262144;
constexpr size_t WS_BYTES = WS_CTL + 4096;
constexpr int AUX_SC1 = 16;
#define GAS __attribute__((address_space(1)))
#define RLX_AGENT __ATOMIC_RELAXED, __HIP_MEMORY_SCOPE_AGENT

__device__ __forceinline__ unsigned pack_bf16(float a, float b) {
    f32x2 v = {a, b};
    bf16x2 r = __builtin_convertvector(v, bf16x2);
    return __builtin_bit_cast(unsigned, r);
}
__device__ __forceinline__ float bf16_round(float a) {
    __bf16 r = (__bf16)a;
    return (float)r;
}
__device__ __forceinline__ f32x4 mfma16(u32x4 a, u32x4 b, f32x4 c) {
    return __builtin_amdgcn_mfma_f32_16x16x32_bf16(__builtin_bit_cast(bf16x8, a), __builtin_bit_cast(bf16x8, b), c, 0, 0, 0);
}
__device__ __forceinline__ f32x4 mfma16h(s16x4 a, u32x2 b, f32x4 c) {
    return __builtin_amdgcn_mfma_f32_16x16x16bf16_1k(a, __builtin_bit_cast(s16x4, b), c, 0, 0, 0);
}
__device__ __forceinline__ unsigned perm_lo(unsigned a, unsigned b) { return __builtin_amdgcn_perm(b, a, 0x05040100u); }
__device__ __forceinline__ unsigned perm_hi(unsigned a, unsigned b) { return __builtin_amdgcn_perm(b, a, 0x07060302u); }
__device__ __forceinline__ unsigned perm_sel(unsigned a, unsigned b, int hi) { return hi ? perm_hi(a, b) : perm_lo(a, b); }
__device__ __forceinline__ unsigned wave_incl_scan(unsigned v) {
    int x = (int)v;
    x += __builtin_amdgcn_update_dpp(0, x, 0x111, 0xF, 0xF, true);
    x += __builtin_amdgcn_update_dpp(0, x, 0x112, 0xF, 0xF, true);
    x += __builtin_amdgcn_update_dpp(0, x, 0x114, 0xF, 0xF, true);
    x += __builtin_amdgcn_update_dpp(0, x, 0x118, 0xF, 0xF, true);
    x += __builtin_amdgcn_update_dpp(0, x, 0x142, 0xA, 0xF, false);
    x += __builtin_amdgcn_update_dpp(0, x, 0x143, 0xC, 0xF, false);
    return (unsigned)x;
}

#define STAMP_DECL
#define STAMP_AT(idx)
#define STAMP_RT(idx)
#define STAMP_ACC(idx)
#define STAMP_FLUSH(wsp, kid)

#define LDS_BARRIER() do { asm volatile("s_waitcnt lgkmcnt(0)" ::: "memory"); __builtin_amdgcn_s_barrier(); asm volatile("" ::: "memory"); } while (0)
__device__ __forceinline__ void dma4(const void* gsrc, void* lds_wave_base) {
    __builtin_amdgcn_global_load_lds((const unsigned*)gsrc, (LAS unsigned*)lds_wave_base, 4, 0, 0);
}
__device__ __forceinline__ void dma16(const void* gsrc, void* lds_wave_base) {
    __builtin_amdgcn_global_load_lds((const unsigned*)gsrc, (LAS unsigned*)lds_wave_base, 16, 0, 0);
}
__device__ __forceinline__ unsigned lds_addr(const void* p) { return (unsigned)(uintptr_t)(LAS const char*)p; }

constexpr int L_Z = 0;
constexpr int L_X = L_Z + T * 2048;
constexpr int L_WA = L_X + 16384;
constexpr int L_P = L_WA + 8192;
constexpr int L_EL = L_P + NG * 2048;
constexpr int L_TOTAL = L_EL + (MAXQ + 2) * 64 + 256;
static_assert(L_TOTAL <= 163840 && L_TOTAL > 81920, "LDS budget: exactly one workgroup per CU");
constexpr int L_PT = L_X + 10240;
constexpr int L_SINK = L_X + 12288;
constexpr int B_ST = L_Z;
constexpr int B_TC = B_ST + NT * CAP * 4 + 256;
constexpr unsigned OOB = 0x80000000u;
#define WIN 24
#define XCOV 1
constexpr unsigned SPIN_CAP = 1u << 17;

__device__ __forceinline__ int zaddr(int node, int ch) {
    return node * 2048 + 16 * (ch ^ ((node ^ (2 * (ch >> 5))) & 15));
}
__device__ __forceinline__ unsigned sw2(unsigned r) { return ((r & 7u) << 1) | (r >> 3); }

__global__ __launch_bounds__(640, 3) void gat_fused(const unsigned short* __restrict__ x, const int* __restrict__ src,
                                                    const int* __restrict__ dst, const unsigned short* __restrict__ w,
                                                    const unsigned short* __restrict__ a_src,
                                                    const unsigned short* __restrict__ a_dst, char* ws,
                                                    unsigned short* __restrict__ out) {
    extern __shared__ __attribute__((aligned(16))) char smem[];
    asm volatile("gat_k2_head_label:");
    const int tid = threadIdx.x, lane = tid & 63;
    const int wv = __builtin_amdgcn_readfirstlane(tid >> 6);
    const int tile = blockIdx.x;
    const int n0 = tile * T;
    unsigned* elist = (unsigned*)(smem + L_EL);
    const int c16 = lane & 15, g = lane >> 4;
    char* gbuf = smem + L_Z + wv * 8192;
    const unsigned gbuf_a = lds_addr(smem) + L_Z + wv * 8192;
#define ABL_X (NN * DD * 2 + 4096)
    const uintptr_t xbase = (uintptr_t)x - 4096;
    const u32x4 xr = {(unsigned)xbase, (unsigned)(xbase >> 32) & 0xFFFFu, (unsigned)(ABL_X), 0x00020000u};
    const unsigned lch = (unsigned)(lane & 31) * 16u;

    STAMP_DECL STAMP_RT(14); STAMP_AT(0);
    unsigned tag_s;
#define TAG_OF(t) do { unsigned e_ = (unsigned)__builtin_amdgcn_readfirstlane((int)epoch_v); \
        e_ = (e_ ^ 0x9E3779B9u) * 0x85EBCA6Bu; e_ ^= e_ >> 15; e_ *= 0xC2B2AE35u; e_ ^= e_ >> 13; t = e_; } while (0)
    const __amdgpu_buffer_rsrc_t wsr = __builtin_amdgcn_make_buffer_rsrc((void*)ws, 0, (int)WS_BYTES, 0x00020000);
    const u32x4 wsr4 = {(unsigned)(uintptr_t)ws, (unsigned)((uintptr_t)ws >> 32) & 0xFFFFu, (unsigned)WS_BYTES, 0x00020000u};
    GAS unsigned* ctl = (GAS unsigned*)(ws + WS_CTL);
    const unsigned epoch_v = __hip_atomic_load(ctl, RLX_AGENT);
    const unsigned own_a = lds_addr(smem) + L_P + wv * 2048;
    {
        const unsigned r0 = (unsigned)(lane >> 5), r1 = r0 + 2u;
        const unsigned o0 = ((unsigned)(n0 + 4 * wv) + r0) * 512u + 4096u + (lch ^ (sw2(r0) << 4));
        const unsigned o1 = ((unsigned)(n0 + 4 * wv) + r1) * 512u + 3072u + (lch ^ (sw2(r1) << 4));
#define OWN_ROWS() do { unsigned keep; \
        asm volatile("s_mov_b32 %0, m0\n\ts_mov_b32 m0, %4\n\ts_nop 0\n\t" \
                     "buffer_load_dwordx4 %1, %3, 0 offen lds\n\tbuffer_load_dwordx4 %2, %3, 0 offen offset:1024 lds\n\t" \
                     "s_mov_b32 m0, %0" : "=&s"(keep) : "v"(o0), "v"(o1), "s"(xr), "s"(own_a) : "memory"); } while (0)
        unsigned tag;
        if (tile < NB) {
            const int c = tile;
            unsigned* stage = (unsigned*)(smem + B_ST);
            unsigned long long* tcnt = (unsigned long long*)(smem + B_TC);
            if (wv < 8) {
                const i32x2* d2 = (const i32x2*)(dst + c * EPB);
                const i32x2* s2 = (const i32x2*)(src + c * EPB);
                i32x2 dv[3], sv[3];
#pragma unroll
                for (int r = 0; r < 3; ++r) {
                    const int i = tid + 512 * r;
                    const int ii = i < EPB / 2 ? i : 0;
                    dv[r] = d2[ii]; sv[r] = s2[ii];
                }
                __builtin_amdgcn_sched_barrier(0);
                if (tid < 256) tcnt[tid] = 0ull;
                LDS_BARRIER();
                __builtin_amdgcn_sched_barrier(0);
                unsigned long long old[6]; unsigned tt[6], dl[6];
#pragma unroll
                for (int k = 0; k < 6; ++k) {
                    const unsigned d = (unsigned)dv[k >> 1][k & 1];
                    tt[k] = d / (unsigned)T;
                    dl[k] = d - tt[k] * T;
                }
#pragma unroll
                for (int k = 0; k < 4; ++k) old[k] = atomicAdd(&tcnt[tt[k]], (1ull << 50) | (1ull << (5 * (dl[k] >> 2))));
                old[4] = old[5] = ~0ull;
                if (tid + 1024 < EPB / 2) {
#pragma unroll
                    for (int k = 4; k < 6; ++k) old[k] = atomicAdd(&tcnt[tt[k]], (1ull << 50) | (1ull << (5 * (dl[k] >> 2))));
                }
                unsigned ent[6];
#pragma unroll
                for (int k = 0; k < 6; ++k) {
                    const unsigned grp = dl[k] >> 2;
                    const unsigned j = (grp * 11u) >> 5, sh = (grp - 3u * j) * 10u;
                    ent[k] = (unsigned)sv[k >> 1][k & 1] | (dl[k] << 14) | (j << 25) | (sh << 27);
                }
                __builtin_amdgcn_sched_barrier(0);
#pragma unroll
                for (int k = 0; k < 6; ++k) {
                    const unsigned pos = (unsigned)(old[k] >> 50);
                    const unsigned rank = (unsigned)(old[k] >> (5 * (dl[k] >> 2))) & 31u;
                    const unsigned idx = pos < (unsigned)CAP ? tt[k] * CAP + pos : (unsigned)(NT * CAP + lane);
                    stage[idx] = ent[k] | (rank << 20);
                }
            } else if (wv == 8) {
                const int ks = c >> 1;
                const int kc = 2 * (c & 1) + (lane >> 5), fc = lane & 31;
                u32x4 r[8];
#pragma unroll
                for (int j = 0; j < 8; ++j) r[j] = *(const u32x4*)(w + (size_t)(32 * ks + 16 * (j >> 2) + 4 * kc + (j & 3)) * DD + 8 * fc);
                __builtin_amdgcn_sched_barrier(0);
                __builtin_amdgcn_s_barrier();
                __builtin_amdgcn_sched_barrier(0);
                const int wq = fc >> 2, gp = fc & 3;
                const int wto = (int)WS_WT + ((wq * 32 + ks) * 2) * 1024 + (kc * 16 + 4 * gp) * 16;
#pragma unroll
                for (int i = 0; i < 8; ++i) {
                    u32x4 o;
#pragma unroll
                    for (int p = 0; p < 4; ++p) o[p] = perm_sel(r[2 * p][i >> 1], r[2 * p + 1][i >> 1], i & 1);
                    __builtin_amdgcn_raw_buffer_store_b128(o, wsr, wto + (i >> 2) * 1024 + (i & 3) * 16, 0, AUX_SC1);
                }
            } else {
                const int h = c >> 4, rb = c & 15;
                const int m = c16, kc = g;
                char* lw = smem + L_X;
                char* la = smem + L_X + 8192;
                dma16((lane < 32 ? a_src : a_dst) + h * DD + (lane & 31) * 8, la);
                const unsigned short* wbase = w + ((size_t)h * DD + 16 * rb) * DD;
#pragma unroll
                for (int i = 0; i < 8; ++i) {
                    const int row = 2 * i + (lane >> 5);
                    dma16(wbase + row * DD + (((lane & 31) ^ row) * 8), lw + i * 1024);
                }
                __builtin_amdgcn_s_barrier();
                asm volatile("s_waitcnt vmcnt(0)" ::: "memory");
                f32x4 acc0 = {0.f, 0.f, 0.f, 0.f}, acc1 = {0.f, 0.f, 0.f, 0.f};
#pragma unroll
                for (int ks = 0; ks < 8; ks += 2) {
                    const u32x4 b0 = *(const u32x4*)(la + (m & 1) * 512 + (4 * ks + kc) * 16);
                    const u32x4 b1 = *(const u32x4*)(la + (m & 1) * 512 + (4 * ks + 4 + kc) * 16);
                    const u32x4 a0 = *(const u32x4*)(lw + m * 512 + (((4 * ks + kc) ^ m) * 16));
                    const u32x4 a1 = *(const u32x4*)(lw + m * 512 + (((4 * ks + 4 + kc) ^ m) * 16));
                    acc0 = mfma16(a0, b0, acc0);
                    acc1 = mfma16(a1, b1, acc1);
                }
                acc0 += acc1;
                u32x2 hi, lo;
                hi[0] = pack_bf16(acc0[0], acc0[1]); hi[1] = pack_bf16(acc0[2], acc0[3]);
                lo[0] = pack_bf16(acc0[0] - __uint_as_float(hi[0] << 16), acc0[1] - __uint_as_float(hi[0] & 0xFFFF0000u));
                lo[1] = pack_bf16(acc0[2] - __uint_as_float(hi[1] << 16), acc0[3] - __uint_as_float(hi[1] & 0xFFFF0000u));
                if (m < 4) {
                    const int d = 16 * rb + 4 * kc, cd = d >> 3;
                    const int ks = cd & 7, kq = ((cd >> 4) & 1) | (((cd >> 3) & 1) << 1);
                    const int mr = 4 * h + 2 * (m & 1) + (m >> 1);
                    __builtin_amdgcn_raw_buffer_store_b64((m >> 1) ? lo : hi, wsr, (int)WS_WA + ks * 1024 + (kq * 16 + mr) * 16 + (d & 7) * 2, 0, AUX_SC1);
                }
            }
            LDS_BARRIER();
            u32x4 v[4]; unsigned tc4[4];
#pragma unroll
            for (int i = 0; i < 4; ++i) {
                const int ii = tid + 640 * i < NT * CAP / 4 ? tid + 640 * i : 0;
                v[i] = ((u32x4*)stage)[ii];
                tc4[i] = ((const unsigned*)tcnt)[(ii >> 3) * 2 + 1] >> 18;
            }
            const unsigned long long cw = tcnt[tid & 255];
#pragma unroll
            for (int i = 0; i < 4; ++i) {
                const int ii = tid + 640 * i;
                if (ii < NT * CAP / 4 && (unsigned)((ii & 7) * 4) < tc4[i])
                    __builtin_amdgcn_raw_buffer_store_b128(v[i], wsr, (int)WS_CELLS + (((ii >> 3) * NB + c) * 8 + (ii & 7)) * 16, 0, AUX_SC1);
            }
            TAG_OF(tag);
            asm volatile("s_waitcnt vmcnt(0)" ::: "memory");
            LDS_BARRIER();
            if (tid < NT) {
                unsigned tc = (unsigned)(cw >> 50);
                const unsigned bad = (tc > 31u || (cw & 0x2108421084210ull) != 0ull) ? 1u : 0u;
                tc = tc > 31u ? 31u : tc;
                u32x4 o;
                o[0] = (unsigned)cw & 0x3FFFFFFFu;
                o[1] = tag;
                o[2] = ((unsigned)(cw >> 30) & 0xFFFFFu) | (tc << 20) | (bad << 28);
                o[3] = tag;
                __builtin_amdgcn_raw_buffer_store_b128(o, wsr, (int)WS_CTAB + (tid * NB + c) * 16, 0, AUX_SC1);
            }
            OWN_ROWS();
        } else {
            OWN_ROWS();
            if (tile < NB + 16 && wv >= 1 && wv < 4) {
                unsigned long long pc; int off, len;
                asm volatile("s_getpc_b64 %0\n.Lpc%=:\n\ts_mov_b32 %1, gat_k2_head_label-.Lpc%=\n\ts_mov_b32 %2, gat_k2_tail_label-gat_k2_head_label"
                             : "=s"(pc), "=s"(off), "=s"(len));
                if ((tid - 64) * 128 < len + 128 && len < 65536) dma4((const char*)(((pc + off) & ~127ull) + (tid - 64) * 128), smem + L_SINK);
            }
            if (wv >= 1) {
#pragma unroll
                for (int k = 0; k < XCOV; ++k) {
                    unsigned line = (unsigned)(tile & 7) * 5000u + (((unsigned)(tile - NB) >> 3) * XCOV + k) * 576u + (unsigned)(tid - 64);
                    line = line >= 40000u ? line - 40000u : line;
                    dma4((const char*)x + (size_t)line * 128, smem + L_SINK);
                }
            }
            TAG_OF(tag);
        }
        tag_s = tag;
    }
    if (tid == 0) __hip_atomic_store(ctl + 64 + tile, tag_s, RLX_AGENT);
    {
        const u32x4 o4 = {OOB, OOB, OOB, OOB}, z4 = {0u, 0u, 0u, 0u};
        if (tid < (MAXQ + 2) * 4) ((u32x4*)elist)[tid] = o4;
#pragma unroll
        for (int i = 0; i < 8; ++i) *(u32x4*)(gbuf + i * 1024 + lane * 16) = z4;
    }
    STAMP_AT(9);
    unsigned* ptab = (unsigned*)(smem + L_PT);
    u32x4 cr = {0u, 0u, 0u, 0u};
    if (wv == 0) {
        const unsigned po = (unsigned)WS_CTAB + (unsigned)(tile * NB + lane) * 16u;
        unsigned spins = 0, ok;
        do {
            asm volatile("buffer_load_dwordx4 %0, %1, %2, 0 offen sc1\n\ts_waitcnt vmcnt(0)" : "=&v"(cr) : "v"(po), "s"(wsr4) : "memory");
            ok = __all(cr[1] == tag_s && cr[3] == tag_s) ? 1u : 0u;
            if (!ok) __builtin_amdgcn_s_sleep(2);
        } while (!ok && ++spins < SPIN_CAP);
        if (lane == 0) ptab[256 + 15] = ok;
    }
    LDS_BARRIER();
    STAMP_AT(10);
    if (__builtin_amdgcn_readfirstlane((int)ptab[256 + 15]) == 0) return;
    u32x4 ce;
    u32x4 wal[4];
    {
        const unsigned co = (unsigned)WS_CELLS + (unsigned)(tile * (NB * CAP / 4) + (tid & 511)) * 16u;
        const unsigned wo = (unsigned)WS_WA + (unsigned)((wv & 1) * 4096 + lane * 16);
        asm volatile("s_cmp_lt_i32 %8, 8\n\ts_cbranch_scc0 .Lw%=\n\tbuffer_load_dwordx4 %0, %5, %7, 0 offen sc1\n\ts_branch .Le%=\n"
                     ".Lw%=:\n\tbuffer_load_dwordx4 %1, %6, %7, 0 offen sc1\n\tbuffer_load_dwordx4 %2, %6, %7, 0 offen offset:1024 sc1\n\t"
                     "buffer_load_dwordx4 %3, %6, %7, 0 offen offset:2048 sc1\n\tbuffer_load_dwordx4 %4, %6, %7, 0 offen offset:3072 sc1\n"
                     ".Le%=:"
                     : "=&v"(ce), "=&v"(wal[0]), "=&v"(wal[1]), "=&v"(wal[2]), "=&v"(wal[3])
                     : "v"(co), "v"(wo), "s"(wsr4), "s"(wv) : "scc", "memory");
    }
    if (wv == 0) {
        u32x4 ct;
        ct[0] = (cr[0] & 15u) | ((cr[0] & (15u << 5)) << 5) | ((cr[0] & (15u << 10)) << 10);
        ct[1] = ((cr[0] >> 15) & 15u) | ((cr[0] >> 10) & (15u << 10)) | ((cr[0] >> 5) & (15u << 20));
        ct[2] = (cr[2] & 15u) | ((cr[2] & (15u << 5)) << 5) | ((cr[2] & (15u << 10)) << 10);
        ct[3] = ((cr[2] >> 15) & 15u) | ((cr[2] >> 10) & (31u << 10)) | ((cr[2] >> 28) << 24);
        u32x4 inc;
#pragma unroll
        for (int k = 0; k < 4; ++k) inc[k] = wave_incl_scan(ct[k]);
        unsigned tot[4], base[4];
#pragma unroll
        for (int k = 0; k < 4; ++k) tot[k] = (unsigned)__builtin_amdgcn_readlane((int)inc[k], 63);
        unsigned run = 0;
#pragma unroll
        for (int k = 0; k < 4; ++k) {
            const unsigned pad = ((tot[k] & (k < 3 ? 0x3FFFFFFFu : 0x3FFu)) + 0x00F03C0Fu) & 0x3F0FC3F0u;
            const unsigned incl = pad * 0x00100401u;
            base[k] = incl - pad + run * 0x00100401u;
            run += (incl >> 20) & 1023u;
        }
        u32x4 ex;
#pragma unroll
        for (int k = 0; k < 4; ++k) ex[k] = inc[k] - ct[k] + base[k];
        ex[3] = (ex[3] & 1023u) | (ct[3] & (63u << 10));
        ((u32x4*)ptab)[lane] = ex;
        if (lane == 0) {
            ((u32x4*)ptab)[64] = u32x4{tot[0], tot[1], tot[2], tot[3]};
            ((u32x4*)ptab)[65] = u32x4{base[0], base[1], base[2], base[3]};
        }
    }
    LDS_BARRIER();
    STAMP_AT(1);
    int qi, left; bool slow;
    {
        const int jw = (wv * 11) >> 5, sh = (wv - 3 * jw) * 10;
        const unsigned t3 = (unsigned)__builtin_amdgcn_readfirstlane((int)ptab[256 + 3]);
        const unsigned tw = (unsigned)__builtin_amdgcn_readfirstlane((int)ptab[256 + jw]);
        const unsigned bw = (unsigned)__builtin_amdgcn_readfirstlane((int)ptab[260 + jw]);
        slow = (t3 >> 24) != 0u || ((t3 >> 10) & 0x3FFFu) > (unsigned)(MAXQ * 16 - 15 * NG);
        qi = slow ? 0 : (int)(((bw >> sh) & 1023u) >> 4);
        left = (int)((tw >> sh) & 1023u);
    }
    const u32x4 pre = ((const u32x4*)ptab)[(tid >> 3) & 63];
    asm volatile("s_waitcnt vmcnt(0)" : "+v"(ce), "+v"(wal[0]), "+v"(wal[1]), "+v"(wal[2]), "+v"(wal[3]) :: "memory");
    if (wv < 8) {
        const unsigned ccnt = slow ? 0u : (pre[3] >> 10);
#pragma unroll
        for (int k = 0; k < 4; ++k) {
            const unsigned e = ce[k];
            const bool j0 = (e >> 25) & 1u, j1 = (e >> 26) & 1u;
            const unsigned lo2 = j0 ? pre[1] : pre[0], hi2 = j0 ? pre[3] : pre[2];
            const unsigned dw = j1 ? hi2 : lo2;
            const unsigned slot = ((dw >> (e >> 27)) & 1023u) + ((e >> 20) & 31u);
            const unsigned r = slot & 15u;
            unsigned pos = (slot & ~15u) | ((r & 1u) << 3) | (r >> 1);
            pos = ((unsigned)((lane & 7) * 4 + k) < ccnt) ? pos : (unsigned)((MAXQ + 2) * 16 + lane);
            elist[pos] = (((e & 0x3FFFu) << 9) + 4096u - ((r & 6u) << 9)) | (sw2(r) << 4) | ((e >> 14) & 3u);
        }
    } else {
#pragma unroll
        for (int k = 0; k < 4; ++k) *(u32x4*)(smem + L_WA + (wv & 1) * 4096 + k * 1024 + lane * 16) = wal[k];
    }
    unsigned rowa = gbuf_a + c16 * 512 + (g & 1) * 256 + ((sw2((unsigned)c16) ^ ((unsigned)(g >> 1) << 3)) << 4);
    const char* wap = smem + L_WA + lane * 16;
#define READ_ROWS() do { asm volatile("" : "+v"(rowa)); \
        _Pragma("unroll") for (int ks = 0; ks < 8; ++ks) xb[ks] = *(const LAS u32x4*)(uintptr_t)(rowa ^ ((unsigned)ks << 4)); } while (0)
#define WAF(k) (*(const u32x4*)(wap + (k) * 1024))
#define LOGITS(LG) do { f32x4 l0 = {0.f, 0.f, 0.f, 0.f}, l1 = {0.f, 0.f, 0.f, 0.f}; \
        _Pragma("unroll") for (int ks = 0; ks < 8; ks += 2) { l0 = mfma16(wa[ks], xb[ks], l0); l1 = mfma16(wa[ks + 1], xb[ks + 1], l1); } \
        LG = l0 + l1; } while (0)
    u32x4 wa[8];
    u32x4 xb[8];
    LDS_BARRIER();
    STAMP_AT(2);
    if (left > 80) __builtin_amdgcn_s_setprio(3); else if (left > 64) __builtin_amdgcn_s_setprio(2); else __builtin_amdgcn_s_setprio(1);
    if (slow) left = 0;
    const int epos = ((c16 & 1) << 3) | (c16 >> 1);
    unsigned fe, ao[8];
#define PREP() do { \
        const u32x4 e0 = *(const u32x4*)(elist + qi * 16 + (lane >> 5) * 8), e1 = *(const u32x4*)(elist + qi * 16 + (lane >> 5) * 8 + 4); \
        fe = elist[qi * 16 + epos]; \
        _Pragma("unroll") for (int k = 0; k < 4; ++k) { ao[k] = (e0[k] & ~15u) ^ lch; ao[4 + k] = (e1[k] & ~15u) ^ lch; } } while (0)
#define ISSUE() do { \
        unsigned keep; \
        asm volatile("s_waitcnt lgkmcnt(0)\n\ts_mov_b32 %0, m0\n\ts_mov_b32 m0, %10\n\t" \
                     "s_cmp_lt_i32 %11, 15\n\ts_cbranch_scc1 .Lp%=\n\t" \
                     "buffer_load_dwordx4 %1, %9, 0 offen lds\n\tbuffer_load_dwordx4 %2, %9, 0 offen offset:1024 lds\n\t" \
                     "buffer_load_dwordx4 %3, %9, 0 offen offset:2048 lds\n\tbuffer_load_dwordx4 %4, %9, 0 offen offset:3072 lds\n\t" \
                     "s_add_u32 m0, m0, 0x1000\n\ts_nop 0\n\t" \
                     "buffer_load_dwordx4 %5, %9, 0 offen lds\n\tbuffer_load_dwordx4 %6, %9, 0 offen offset:1024 lds\n\t" \
                     "buffer_load_dwordx4 %7, %9, 0 offen offset:2048 lds\n\tbuffer_load_dwordx4 %8, %9, 0 offen offset:3072 lds\n\t" \
                     "s_branch .Lq%=\n.Lp%=:\n\t" \
                     "s_cmp_lt_i32 %11, 1\n\ts_cbranch_scc1 .Lq%=\n\tbuffer_load_dwordx4 %1, %9, 0 offen lds\n\t" \
                     "s_cmp_lt_i32 %11, 3\n\ts_cbranch_scc1 .Lq%=\n\tbuffer_load_dwordx4 %2, %9, 0 offen offset:1024 lds\n\t" \
                     "s_cmp_lt_i32 %11, 5\n\ts_cbranch_scc1 .Lq%=\n\tbuffer_load_dwordx4 %3, %9, 0 offen offset:2048 lds\n\t" \
                     "s_cmp_lt_i32 %11, 7\n\ts_cbranch_scc1 .Lq%=\n\tbuffer_load_dwordx4 %4, %9, 0 offen offset:3072 lds\n\t" \
                     "s_add_u32 m0, m0, 0x1000\n\t" \
                     "s_cmp_lt_i32 %11, 9\n\ts_cbranch_scc1 .Lq%=\n\tbuffer_load_dwordx4 %5, %9, 0 offen lds\n\t" \
                     "s_cmp_lt_i32 %11, 11\n\ts_cbranch_scc1 .Lq%=\n\tbuffer_load_dwordx4 %6, %9, 0 offen offset:1024 lds\n\t" \
                     "s_cmp_lt_i32 %11, 13\n\ts_cbranch_scc1 .Lq%=\n\tbuffer_load_dwordx4 %7, %9, 0 offen offset:2048 lds\n" \
                     ".Lq%=:\n\ts_mov_b32 m0, %0" \
                     : "=&s"(keep) \
                     : "v"(ao[0]), "v"(ao[1]), "v"(ao[2]), "v"(ao[3]), "v"(ao[4]), "v"(ao[5]), "v"(ao[6]), "v"(ao[7]), \
                       "s"(xr), "s"(gbuf_a), "s"(left) : "scc", "memory"); } while (0)
    PREP();
    ISSUE();
    float sdq;
    {
        const unsigned oa = own_a + (c16 & 3) * 512 + (g & 1) * 256 + ((sw2((unsigned)(c16 & 3)) ^ ((unsigned)(g >> 1) << 3)) << 4);
#pragma unroll
        for (int ks = 0; ks < 8; ++ks) xb[ks] = *(const LAS u32x4*)(uintptr_t)(oa ^ ((unsigned)ks << 4));
#pragma unroll
        for (int k = 0; k < 8; ++k) wa[k] = WAF(k);
        f32x4 lg; LOGITS(lg); sdq = lg[2] + lg[3];
    }
    STAMP_AT(3); STAMP_AT(4); STAMP_AT(13);

    if (!slow) {
        const s16x4 ones2 = {0x3F80, 0x3F80, 0x3F80, 0x3F80};
        char* pscr = smem + L_P + wv * 2048;
        f32x4 acc[16], accd = {0.f, 0.f, 0.f, 0.f};
#pragma unroll
        for (int i = 0; i < 16; ++i) acc[i] = f32x4{0.f, 0.f, 0.f, 0.f};
        const unsigned trow = 4u * g + ((unsigned)c16 >> 2), tp = (unsigned)c16 & 3u;
        unsigned tra = gbuf_a + trow * 512 + (tp & 1u) * 8 + ((sw2(trow) ^ (tp >> 1)) << 4);
        *(u32x2*)(pscr + lane * 8) = u32x2{0u, 0u};
#pragma unroll 1
        while (left > 0) {
            left -= 16; qi += 1;
            const unsigned fc = fe;
            const float sdl = __builtin_bit_cast(float, __builtin_amdgcn_ds_bpermute((int)((g * 16 + (fc & 3u)) * 4), __builtin_bit_cast(int, sdq)));
            asm volatile("s_waitcnt vmcnt(0)" ::: "memory");
            asm volatile("" : "+v"(rowa));
            f32x4 l0 = {0.f, 0.f, 0.f, 0.f}, l1 = {0.f, 0.f, 0.f, 0.f};
#define XR(ks) (*(const LAS u32x4*)(uintptr_t)(rowa ^ ((unsigned)(ks) << 4)))
            {
                const u32x4 x0 = XR(0), x1 = XR(1), x2 = XR(2), x3 = XR(3), x4 = XR(4), x5 = XR(5);
                __builtin_amdgcn_sched_barrier(0);
                l0 = mfma16(wa[0], x0, l0); l1 = mfma16(wa[1], x1, l1);
                l0 = mfma16(wa[2], x2, l0); l1 = mfma16(wa[3], x3, l1);
                __builtin_amdgcn_sched_barrier(0);
                const u32x4 x6 = XR(6), x7 = XR(7);
                __builtin_amdgcn_sched_barrier(0);
                l0 = mfma16(wa[4], x4, l0); l1 = mfma16(wa[5], x5, l1);
                l0 = mfma16(wa[6], x6, l0); l1 = mfma16(wa[7], x7, l1);
                __builtin_amdgcn_sched_barrier(0);
            }
            s16x4 tf[16];
            asm volatile("" : "+v"(tra));
#pragma unroll
            for (int t = 0; t < 8; ++t) {
                const unsigned ta = tra ^ ((unsigned)t << 5);
                tf[t] = __builtin_amdgcn_ds_read_tr16_b64_v4i16((LAS s16x4*)(uintptr_t)ta);
                tf[t + 8] = __builtin_amdgcn_ds_read_tr16_b64_v4i16((LAS s16x4*)(uintptr_t)(ta + 256));
            }
            __builtin_amdgcn_sched_barrier(0);
            float e = ((l0[0] + l1[0]) + (l0[1] + l1[1])) + sdl;
            e = e > 0.f ? e : 0.2f * e;
            float p = __expf(e);
            p = fc != OOB ? p : 0.f;
            __builtin_amdgcn_sched_barrier(0);
            PREP();
            *(__bf16*)(pscr + ((c16 >> 2) * 16 + (fc & 3u) * 4 + g) * 8 + (c16 & 3) * 2) = (__bf16)p;
            const u32x2 pf = *(const u32x2*)(pscr + lane * 8);
            *(u32x2*)(pscr + lane * 8) = u32x2{0u, 0u};
            ISSUE();
#pragma unroll
            for (int t = 0; t < 16; ++t) acc[t] = mfma16h(tf[t], pf, acc[t]);
            accd = mfma16h(ones2, pf, accd);
        }
        {
            const float sc = 0.25f * __builtin_amdgcn_rcpf(accd[0] + 1e-9f);
            const int node = wv * 4 + (c16 >> 2), h = c16 & 3;
#pragma unroll
            for (int u = 0; u < 8; ++u) {
                const f32x4 a = acc[2 * u] * sc, b2 = acc[2 * u + 1] * sc;
                u32x4 o;
                o[0] = pack_bf16(a[0], a[1]); o[1] = pack_bf16(a[2], a[3]);
                o[2] = pack_bf16(b2[0], b2[1]); o[3] = pack_bf16(b2[2], b2[3]);
                *(u32x4*)(smem + L_Z + zaddr(node, h * 32 + 4 * u + g)) = o;
            }
        }
    } else {
        float wsv[4][4];
        {
            const int d = 4 * lane, cd = d >> 3;
            const int ks = cd & 7, kq = ((cd >> 4) & 1) | (((cd >> 3) & 1) << 1);
#pragma unroll
            for (int h = 0; h < 4; ++h) {
                const u32x2 hi = *(const u32x2*)(smem + L_WA + ks * 1024 + (kq * 16 + 4 * h) * 16 + (d & 7) * 2);
                const u32x2 lo = *(const u32x2*)(smem + L_WA + ks * 1024 + (kq * 16 + 4 * h + 1) * 16 + (d & 7) * 2);
                wsv[h][0] = __uint_as_float(hi[0] << 16) + __uint_as_float(lo[0] << 16);
                wsv[h][1] = __uint_as_float(hi[0] & 0xFFFF0000u) + __uint_as_float(lo[0] & 0xFFFF0000u);
                wsv[h][2] = __uint_as_float(hi[1] << 16) + __uint_as_float(lo[1] << 16);
                wsv[h][3] = __uint_as_float(hi[1] & 0xFFFF0000u) + __uint_as_float(lo[1] & 0xFFFF0000u);
            }
        }
#pragma unroll 1
        for (int nn = 0; nn < 4; ++nn) {
            const int nl = wv * 4 + nn;
            const int node = n0 + nl;
            float acc[4][4], den[4], sdn[4];
#pragma unroll
            for (int h = 0; h < 4; ++h) { den[h] = 0.f; sdn[h] = __shfl(sdq, h * 16 + nn, 64);
#pragma unroll
                for (int i = 0; i < 4; ++i) acc[h][i] = 0.f; }
#pragma unroll 1
            for (int e0 = 0; e0 < NE; e0 += 64) {
                const int e = e0 + lane;
                const int dv = dst[e];
                const int sv = src[e];
                unsigned long long mask = __ballot(dv == node);
                while (mask) {
                    const int bit = __builtin_ctzll(mask);
                    mask &= mask - 1;
                    const int s = __shfl(sv, bit, 64);
                    const uint2 xv = *(const uint2*)(x + (size_t)s * DD + lane * 4);
                    const float xf[4] = {__uint_as_float(xv.x << 16), __uint_as_float(xv.x & 0xFFFF0000u),
                                         __uint_as_float(xv.y << 16), __uint_as_float(xv.y & 0xFFFF0000u)};
#pragma unroll
                    for (int h = 0; h < 4; ++h) {
                        float dsum = (xf[0] * wsv[h][0] + xf[1] * wsv[h][1]) + (xf[2] * wsv[h][2] + xf[3] * wsv[h][3]);
#pragma unroll
                        for (int o = 32; o > 0; o >>= 1) dsum += __shfl_xor(dsum, o, 64);
                        float ev = dsum + sdn[h];
                        ev = ev > 0.f ? ev : 0.2f * ev;
                        const float p = bf16_round(__expf(ev));
                        den[h] += p;
#pragma unroll
                        for (int i = 0; i < 4; ++i) acc[h][i] += p * xf[i];
                    }
                }
            }
#pragma unroll
            for (int h = 0; h < 4; ++h) {
                const float sc = 0.25f / (den[h] + 1e-9f);
                uint2 o;
                o.x = pack_bf16(acc[h][0] * sc, acc[h][1] * sc);
                o.y = pack_bf16(acc[h][2] * sc, acc[h][3] * sc);
                *(uint2*)(smem + L_Z + zaddr(nl, h * 32 + 4 * (lane >> 3) + (lane & 3)) + ((lane >> 2) & 1) * 8) = o;
            }
        }
    }
    asm volatile("s_waitcnt vmcnt(0)" ::: "memory");
    __builtin_amdgcn_s_setprio(0);
    STAMP_AT(5);
    if (wv >= 8) {
        LDS_BARRIER();
        if (tile == 0 && wv == 8) {
            unsigned spins = 0, ok;
            do {
                bool good = true;
#pragma unroll
                for (int k = 0; k < 4; ++k) {
                    const int i = lane + 64 * k;
                    good = good && (i >= NT || __hip_atomic_load(ctl + 64 + (i < NT ? i : 0), RLX_AGENT) == tag_s);
                }
                ok = __all(good) ? 1u : 0u;
                if (!ok) __builtin_amdgcn_s_sleep(8);
            } while (!ok && ++spins < SPIN_CAP);
            if (lane == 0) __hip_atomic_store(ctl, (unsigned)__builtin_amdgcn_readfirstlane((int)epoch_v) + 1u, RLX_AGENT);
        }
        STAMP_AT(6); STAMP_AT(7); STAMP_AT(8); STAMP_RT(15); STAMP_FLUSH(ws, 1);
        return;
    }
    const int wto = (int)WS_WT + wv * 65536 + lane * 16;
#define WLD(i) __builtin_amdgcn_raw_buffer_load_b128(wsr, wto + ((i) & 3) * 1024, ((i) >> 2) * 4096, AUX_SC1)
    u32x4 wr[WIN];
#pragma unroll
    for (int i = 0; i < WIN; ++i) wr[i] = WLD(i);
    __builtin_amdgcn_sched_barrier(0);
    LDS_BARRIER();
    STAMP_AT(6);
    {
        f32x4 acc[2][3];
#pragma unroll
        for (int t = 0; t < 2; ++t)
#pragma unroll
            for (int nt = 0; nt < 3; ++nt) acc[t][nt] = f32x4{0.f, 0.f, 0.f, 0.f};
        u32x4 zf[2][3];
#pragma unroll
        for (int nt = 0; nt < 3; ++nt) zf[0][nt] = *(const u32x4*)(smem + L_Z + zaddr(nt * 16 + c16, g));
#pragma unroll
        for (int ks = 0; ks < 32; ++ks) {
            if (ks + 1 < 32) {
#pragma unroll
                for (int nt = 0; nt < 3; ++nt)
                    zf[(ks + 1) & 1][nt] = *(const u32x4*)(smem + L_Z + zaddr(nt * 16 + c16, 4 * (ks + 1) + g));
            }
            const u32x4 w0 = wr[(ks * 2) % WIN], w1 = wr[(ks * 2 + 1) % WIN];
#pragma unroll
            for (int nt = 0; nt < 3; ++nt) {
                acc[0][nt] = mfma16(w0, zf[ks & 1][nt], acc[0][nt]);
                acc[1][nt] = mfma16(w1, zf[ks & 1][nt], acc[1][nt]);
            }
            if (ks * 2 + WIN < 64) {
                wr[(ks * 2) % WIN] = WLD(ks * 2 + WIN);
                wr[(ks * 2 + 1) % WIN] = WLD(ks * 2 + 1 + WIN);
            }
            __builtin_amdgcn_sched_barrier(0);
        }
        STAMP_AT(7);
#pragma unroll
        for (int nt = 0; nt < 3; ++nt) {
            const int nl = nt * 16 + c16;
            if (nl < T) {
                u32x4 o;
                o[0] = pack_bf16(acc[0][nt][0], acc[0][nt][1]);
                o[1] = pack_bf16(acc[0][nt][2], acc[0][nt][3]);
                o[2] = pack_bf16(acc[1][nt][0], acc[1][nt][1]);
                o[3] = pack_bf16(acc[1][nt][2], acc[1][nt][3]);
                *(u32x4*)(out + (size_t)(n0 + nl) * DD + 32 * wv + 8 * g) = o;
            }
        }
    }
    STAMP_AT(8); STAMP_RT(15); STAMP_FLUSH(ws, 1);
    asm volatile("gat_k2_tail_label:");
}

extern "C" void kernel_launch(void* const* d_in, const int* in_sizes, int n_in, void* d_out, int out_size,
                              void* d_ws, size_t ws_size, hipStream_t stream) {
    const unsigned short* x = (const unsigned short*)d_in[0];
    const int* src = (const int*)d_in[1];
    const int* dst = (const int*)d_in[2];
    const unsigned short* w = (const unsigned short*)d_in[3];
    const unsigned short* a_src = (const unsigned short*)d_in[4];
    const unsigned short* a_dst = (const unsigned short*)d_in[5];
    (void)hipFuncSetAttribute((const void*)gat_fused, hipFuncAttributeMaxDynamicSharedMemorySize, L_TOTAL);
    gat_fused<<<NT, 640, L_TOTAL, stream>>>(x, src, dst, w, a_src, a_dst, (char*)d_ws, (unsigned short*)d_out);
}
```

```cpp
#include <hip/hip_runtime.h>
#include <hip/hip_bf16.h>
#include <stdint.h>

typedef __attribute__((ext_vector_type(8))) __bf16 bf16x8;
typedef __attribute__((ext_vector_type(2))) __bf16 bf16x2;
typedef __attribute__((ext_vector_type(4))) short s16x4;
typedef __attribute__((ext_vector_type(4))) float f32x4;
typedef __attribute__((ext_vector_type(2))) float f32x2;
typedef __attribute__((ext_vector_type(4))) unsigned u32x4;
typedef __attribute__((ext_vector_type(2))) unsigned u32x2;
typedef __attribute__((ext_vector_type(2))) int i32x2;
#define LAS __attribute__((address_space(3)))

constexpr int NN = 10000, NE = 160000, DD = 256, HH = 4;
constexpr int T = 40, NT = NN / T;
constexpr int NB = 64, CAP = 32, EPB = NE / NB;
constexpr int NG = T / 4;
constexpr int MAXQ = 58;

constexpr size_t WS_CELLS = 0;
constexpr size_t WS_WT    = 2097152;
constexpr size_t WS_WA    = WS_WT + 524288;
constexpr size_t WS_CTAB  = WS_WA + 8192;

__device__ __forceinline__ unsigned pack_bf16(float a, float b) {
    f32x2 v = {a, b};
    bf16x2 r = __builtin_convertvector(v, bf16x2);
    return __builtin_bit_cast(unsigned, r);
}
__device__ __forceinline__ float bf16_round(float a) {
    __bf16 r = (__bf16)a;
    return (float)r;
}
__device__ __forceinline__ f32x4 mfma16(u32x4 a, u32x4 b, f32x4 c) {
    return __builtin_amdgcn_mfma_f32_16x16x32_bf16(__builtin_bit_cast(bf16x8, a), __builtin_bit_cast(bf16x8, b), c, 0, 0, 0);
}
__device__ __forceinline__ f32x4 mfma16h(s16x4 a, u32x2 b, f32x4 c) {
    return __builtin_amdgcn_mfma_f32_16x16x16bf16_1k(a, __builtin_bit_cast(s16x4, b), c, 0, 0, 0);
}
__device__ __forceinline__ unsigned perm_lo(unsigned a, unsigned b) { return __builtin_amdgcn_perm(b, a, 0x05040100u); }
__device__ __forceinline__ unsigned perm_hi(unsigned a, unsigned b) { return __builtin_amdgcn_perm(b, a, 0x07060302u); }
__device__ __forceinline__ unsigned perm_sel(unsigned a, unsigned b, int hi) { return hi ? perm_hi(a, b) : perm_lo(a, b); }
__device__ __forceinline__ unsigned wave_incl_scan(unsigned v) {
    int x = (int)v;
    x += __builtin_amdgcn_update_dpp(0, x, 0x111, 0xF, 0xF, true);
    x += __builtin_amdgcn_update_dpp(0, x, 0x112, 0xF, 0xF, true);
    x += __builtin_amdgcn_update_dpp(0, x, 0x114, 0xF, 0xF, true);
    x += __builtin_amdgcn_update_dpp(0, x, 0x118, 0xF, 0xF, true);
    x += __builtin_amdgcn_update_dpp(0, x, 0x142, 0xA, 0xF, false);
    x += __builtin_amdgcn_update_dpp(0, x, 0x143, 0xC, 0xF, false);
    return (unsigned)x;
}

#define LDS_BARRIER() do { asm volatile("s_waitcnt lgkmcnt(0)" ::: "memory"); __builtin_amdgcn_s_barrier(); asm volatile("" ::: "memory"); } while (0)
__device__ __forceinline__ void dma4(const void* gsrc, void* lds_wave_base) {
    __builtin_amdgcn_global_load_lds((const unsigned*)gsrc, (LAS unsigned*)lds_wave_base, 4, 0, 0);
}
__device__ __forceinline__ void dma16(const void* gsrc, void* lds_wave_base) {
    __builtin_amdgcn_global_load_lds((const unsigned*)gsrc, (LAS unsigned*)lds_wave_base, 16, 0, 0);
}
__device__ __forceinline__ unsigned lds_addr(const void* p) { return (unsigned)(uintptr_t)(LAS const char*)p; }

constexpr int K1L_ST = 0;
constexpr int K1L_TC = NT * CAP * 4 + 256;
constexpr int K1L_DUMP = K1L_TC + 2048;
constexpr int K1L_TOTAL = K1L_DUMP + 256;
#define K1COV 512

__global__ __launch_bounds__(512, 2) void gat_k1(const unsigned short* __restrict__ x, const int* __restrict__ src,
                                                 const int* __restrict__ dst, const unsigned short* __restrict__ w,
                                                 const unsigned short* __restrict__ a_src,
                                                 const unsigned short* __restrict__ a_dst, char* __restrict__ ws) {
    extern __shared__ __attribute__((aligned(16))) char smem[];
    const int tid = threadIdx.x, lane = tid & 63;
    const int wv = __builtin_amdgcn_readfirstlane(tid >> 6);
    const int b = blockIdx.x;
    if (b >= NB) {
        if (b < NB + 64 && wv == 0) {
            const int task = b - NB, h = task >> 4, rb = task & 15;
            const int m = lane & 15, kc = lane >> 4;
            char* lw = smem;
            char* la = smem + 8192;
            dma16((lane < 32 ? a_src : a_dst) + h * DD + (lane & 31) * 8, la);
            const unsigned short* wbase = w + ((size_t)h * DD + 16 * rb) * DD;
#pragma unroll
            for (int i = 0; i < 8; ++i) {
                const int row = 2 * i + (lane >> 5);
                dma16(wbase + row * DD + (((lane & 31) ^ row) * 8), lw + i * 1024);
            }
            asm volatile("s_waitcnt vmcnt(0)" ::: "memory");
            f32x4 acc0 = {0.f, 0.f, 0.f, 0.f}, acc1 = {0.f, 0.f, 0.f, 0.f};
#pragma unroll
            for (int ks = 0; ks < 8; ks += 2) {
                const u32x4 b0 = *(const u32x4*)(la + (m & 1) * 512 + (4 * ks + kc) * 16);
                const u32x4 b1 = *(const u32x4*)(la + (m & 1) * 512 + (4 * ks + 4 + kc) * 16);
                const u32x4 a0 = *(const u32x4*)(lw + m * 512 + (((4 * ks + kc) ^ m) * 16));
                const u32x4 a1 = *(const u32x4*)(lw + m * 512 + (((4 * ks + 4 + kc) ^ m) * 16));
                acc0 = mfma16(a0, b0, acc0);
                acc1 = mfma16(a1, b1, acc1);
            }
            acc0 += acc1;
            u32x2 hi, lo;
            hi[0] = pack_bf16(acc0[0], acc0[1]); hi[1] = pack_bf16(acc0[2], acc0[3]);
            lo[0] = pack_bf16(acc0[0] - __uint_as_float(hi[0] << 16), acc0[1] - __uint_as_float(hi[0] & 0xFFFF0000u));
            lo[1] = pack_bf16(acc0[2] - __uint_as_float(hi[1] << 16), acc0[3] - __uint_as_float(hi[1] & 0xFFFF0000u));
            if (m < 4) {
                const int d = 16 * rb + 4 * kc, cd = d >> 3;
                const int ks = cd & 7, kq = ((cd >> 4) & 1) | (((cd >> 3) & 1) << 1);
                const int mr = 4 * h + 2 * (m & 1) + (m >> 1);
                *(u32x2*)(ws + WS_WA + ks * 1024 + (kq * 16 + mr) * 16 + (d & 7) * 2) = (m >> 1) ? lo : hi;
            }
        } else if (b >= NB + 64 && b < NB + 80 && wv < 3) {
            unsigned long long pc; int off, len;
            asm volatile("s_getpc_b64 %0\n.Lpc%=:\n\ts_mov_b32 %1, gat_k2_head_label-.Lpc%=\n\ts_mov_b32 %2, gat_k2_tail_label-gat_k2_head_label"
                         : "=s"(pc), "=s"(off), "=s"(len));
            if (tid * 128 < len + 128 && len < 65536) dma4((const char*)(((pc + off) & ~127ull) + tid * 128), smem + K1L_DUMP);
        }
        if (tid < K1COV) {
            unsigned line = (unsigned)(b & 7) * 5000u + ((unsigned)(b - NB) >> 3) * K1COV + (unsigned)tid;
            line = line >= 40000u ? line - 40000u : line;
            dma4((const char*)x + (size_t)line * 128, smem + K1L_DUMP);
        }
    } else {
        const int c = b;
        unsigned* stage = (unsigned*)(smem + K1L_ST);
        unsigned long long* tcnt = (unsigned long long*)(smem + K1L_TC);
        if (wv < 7) {
            const i32x2* d2 = (const i32x2*)(dst + c * EPB);
            const i32x2* s2 = (const i32x2*)(src + c * EPB);
            i32x2 dv[3], sv[3];
#pragma unroll
            for (int r = 0; r < 3; ++r) {
                const int i = tid + 448 * r;
                const int ii = i < EPB / 2 ? i : 0;
                dv[r] = d2[ii]; sv[r] = s2[ii];
            }
            __builtin_amdgcn_sched_barrier(0);
            if (tid < 256) tcnt[tid] = 0ull;
            LDS_BARRIER();
            __builtin_amdgcn_sched_barrier(0);
            unsigned long long old[6]; unsigned tt[6], dl[6];
#pragma unroll
            for (int k = 0; k < 6; ++k) {
                const unsigned d = (unsigned)dv[k >> 1][k & 1];
                tt[k] = d / (unsigned)T;
                dl[k] = d - tt[k] * T;
            }
#pragma unroll
            for (int k = 0; k < 4; ++k) old[k] = atomicAdd(&tcnt[tt[k]], (1ull << 50) | (1ull << (5 * (dl[k] >> 2))));
            old[4] = old[5] = ~0ull;
            if (tid + 896 < EPB / 2) {
#pragma unroll
                for (int k = 4; k < 6; ++k) old[k] = atomicAdd(&tcnt[tt[k]], (1ull << 50) | (1ull << (5 * (dl[k] >> 2))));
            }
            unsigned ent[6];
#pragma unroll
            for (int k = 0; k < 6; ++k) {
                const unsigned grp = dl[k] >> 2;
                const unsigned j = (grp * 11u) >> 5, sh = (grp - 3u * j) * 10u;
                ent[k] = (unsigned)sv[k >> 1][k & 1] | (dl[k] << 14) | (j << 25) | (sh << 27);
            }
            __builtin_amdgcn_sched_barrier(0);
#pragma unroll
            for (int k = 0; k < 6; ++k) {
                const unsigned pos = (unsigned)(old[k] >> 50);
                const unsigned rank = (unsigned)(old[k] >> (5 * (dl[k] >> 2))) & 31u;
                const unsigned idx = pos < (unsigned)CAP ? tt[k] * CAP + pos : (unsigned)(NT * CAP + lane);
                stage[idx] = ent[k] | (rank << 20);
            }
        } else {
            const int ks = c >> 1;
            const int kc = 2 * (c & 1) + (lane >> 5), fc = lane & 31;
            u32x4 r[8];
#pragma unroll
            for (int j = 0; j < 8; ++j) r[j] = *(const u32x4*)(w + (size_t)(32 * ks + 16 * (j >> 2) + 4 * kc + (j & 3)) * DD + 8 * fc);
            __builtin_amdgcn_sched_barrier(0);
            __builtin_amdgcn_s_barrier();
            __builtin_amdgcn_sched_barrier(0);
            const int wq = fc >> 2, gp = fc & 3;
            char* wt = ws + WS_WT + (size_t)((wq * 32 + ks) * 2) * 1024 + (kc * 16 + 4 * gp) * 16;
#pragma unroll
            for (int i = 0; i < 8; ++i) {
                u32x4 o;
#pragma unroll
                for (int p = 0; p < 4; ++p) o[p] = perm_sel(r[2 * p][i >> 1], r[2 * p + 1][i >> 1], i & 1);
                *(u32x4*)(wt + (i >> 2) * 1024 + (i & 3) * 16) = o;
            }
        }
        LDS_BARRIER();
        u32x4* cells4 = (u32x4*)(ws + WS_CELLS);
        u32x4 v[4]; unsigned tc4[4];
#pragma unroll
        for (int i = 0; i < 4; ++i) {
            const int ii = tid + 512 * i < NT * CAP / 4 ? tid + 512 * i : 0;
            v[i] = ((u32x4*)stage)[ii];
            tc4[i] = ((const unsigned*)tcnt)[(ii >> 3) * 2 + 1] >> 18;
        }
        const unsigned long long cw = tcnt[tid & 255];
#pragma unroll
        for (int i = 0; i < 4; ++i) {
            const int ii = tid + 512 * i;
            if (ii < NT * CAP / 4 && (unsigned)((ii & 7) * 4) < tc4[i]) cells4[((ii >> 3) * NB + c) * 8 + (ii & 7)] = v[i];
        }
        if (tid < NT) {
            unsigned tc = (unsigned)(cw >> 50);
            const unsigned bad = (tc > 31u || (cw & 0x2108421084210ull) != 0ull) ? 1u : 0u;
            tc = tc > 31u ? 31u : tc;
            unsigned gc[NG];
#pragma unroll
            for (int k = 0; k < NG; ++k) gc[k] = (unsigned)(cw >> (5 * k)) & 15u;
            u32x4 o;
            o[0] = gc[0] | (gc[1] << 10) | (gc[2] << 20);
            o[1] = gc[3] | (gc[4] << 10) | (gc[5] << 20);
            o[2] = gc[6] | (gc[7] << 10) | (gc[8] << 20);
            o[3] = gc[9] | (tc << 10) | (bad << 24);
            ((u32x4*)(ws + WS_CTAB))[tid * NB + c] = o;
        }
    }
    asm volatile("s_waitcnt vmcnt(0)" ::: "memory");
}

constexpr int NLF = 6;
constexpr int L_Z = 0;
constexpr int L_W = L_Z + T * 2048;
constexpr int L_WA = L_W + 8 * NLF * 1024;
constexpr int L_P = L_WA + 8192;
constexpr int L_EL = L_P + NG * 2048;
constexpr int L_TOTAL = L_EL + (MAXQ + 2) * 64 + 256;
static_assert(L_TOTAL <= 163840, "LDS budget");
constexpr unsigned OOB = 0x80000000u;
#define WIN 24

__device__ __forceinline__ int zaddr(int node, int ch) {
    return node * 2048 + 16 * (ch ^ ((node ^ (2 * (ch >> 5))) & 15));
}
__device__ __forceinline__ unsigned sw2(unsigned r) { return ((r & 7u) << 1) | (r >> 3); }

__global__ __launch_bounds__(640, 3) void gat_k2(const unsigned short* __restrict__ x, const int* __restrict__ src,
                                                 const int* __restrict__ dst, const char* __restrict__ ws,
                                                 unsigned short* __restrict__ out) {
    extern __shared__ __attribute__((aligned(16))) char smem[];
    asm volatile("gat_k2_head_label:");
    const int tid = threadIdx.x, lane = tid & 63;
    const int wv = __builtin_amdgcn_readfirstlane(tid >> 6);
    const int tile = blockIdx.x;
    const int n0 = tile * T;
    unsigned* elist = (unsigned*)(smem + L_EL);
    const int c16 = lane & 15, g = lane >> 4;
    char* gbuf = smem + L_Z + wv * 8192;
    const unsigned gbuf_a = lds_addr(smem) + L_Z + wv * 8192;
    const uintptr_t xbase = (uintptr_t)x - 4096;
    const u32x4 xr = {(unsigned)xbase, (unsigned)(xbase >> 32) & 0xFFFFu, (unsigned)(NN * DD * 2 + 4096), 0x00020000u};
    const unsigned lch = (unsigned)(lane & 31) * 16u;

#define DMA2(gp, la) do { unsigned keep_; \
        asm volatile("s_mov_b32 %0, m0\n\ts_mov_b32 m0, %2\n\ts_nop 0\n\t" \
                     "global_load_lds_dwordx4 %1, off\n\tglobal_load_lds_dwordx4 %1, off offset:1024\n\t" \
                     "s_mov_b32 m0, %0" : "=&s"(keep_) : "v"(gp), "s"(la) : "memory"); } while (0)
    u32x4 ct, ce;
    const unsigned own_a = lds_addr(smem) + L_P + wv * 2048;
    {
        const u32x4* pct = (const u32x4*)(ws + WS_CTAB) + tile * NB + lane;
        const u32x4* pce = (const u32x4*)(ws + WS_CELLS) + tile * (NB * CAP / 4) + (tid & 511);
        asm volatile("global_load_dwordx4 %0, %2, off\n\tglobal_load_dwordx4 %1, %3, off"
                     : "=&v"(ct), "=&v"(ce) : "v"(pct), "v"(pce) : "memory");
    }
    if (wv >= 8) {
        const char* wap = ws + WS_WA + (wv - 8) * 4096 + lane * 16;
        const unsigned wal = lds_addr(smem) + L_WA + (wv - 8) * 4096;
        DMA2(wap, wal); DMA2(wap + 2048, wal + 2048);
    }
    {
        const u32x4 o4 = {OOB, OOB, OOB, OOB}, z4 = {0u, 0u, 0u, 0u};
        if (tid < (MAXQ + 2) * 4) ((u32x4*)elist)[tid] = o4;
#pragma unroll
        for (int i = 0; i < 8; ++i) *(u32x4*)(gbuf + i * 1024 + lane * 16) = z4;
    }
    LDS_BARRIER();
    {
        const unsigned r0 = (unsigned)(lane >> 5), r1 = r0 + 2u;
        const unsigned o0 = ((unsigned)(n0 + 4 * wv) + r0) * 512u + 4096u + (lch ^ (sw2(r0) << 4));
        const unsigned o1 = ((unsigned)(n0 + 4 * wv) + r1) * 512u + 3072u + (lch ^ (sw2(r1) << 4));
        unsigned keep;
        asm volatile("s_mov_b32 %0, m0\n\ts_mov_b32 m0, %4\n\ts_nop 0\n\t"
                     "buffer_load_dwordx4 %1, %3, 0 offen lds\n\tbuffer_load_dwordx4 %2, %3, 0 offen offset:1024 lds\n\t"
                     "s_mov_b32 m0, %0"
                     : "=&s"(keep) : "v"(o0), "v"(o1), "s"(xr), "s"(own_a) : "memory");
    }
    if (wv >= 8) {
        const char* wsrc = ws + WS_WT + (size_t)(wv - 8) * 4 * 65536 + lane * 16;
        const unsigned wl = lds_addr(smem) + L_W + (wv - 8) * 4 * NLF * 1024;
#pragma unroll
        for (int i = 0; i < 4 * NLF; i += 2) DMA2(wsrc + (i / NLF) * 65536 + (i % NLF) * 1024, wl + i * 1024);
    }
    asm volatile("s_cmp_lt_i32 %2, 8\n\ts_cbranch_scc1 .Lwa%=\n\ts_waitcnt vmcnt(%3)\n\ts_branch .Lwb%=\n"
                 ".Lwa%=:\n\ts_waitcnt vmcnt(2)\n.Lwb%=:"
                 : "+v"(ct), "+v"(ce) : "s"(wv), "i"(4 * NLF + 2) : "scc", "memory");
    u32x4 inc;
#pragma unroll
    for (int k = 0; k < 4; ++k) inc[k] = wave_incl_scan(ct[k]);
    unsigned tot[4];
#pragma unroll
    for (int k = 0; k < 4; ++k) tot[k] = (unsigned)__builtin_amdgcn_readlane((int)inc[k], 63);
    const bool slow = (tot[3] >> 24) != 0u || ((tot[3] >> 10) & 0x3FFFu) > (unsigned)(MAXQ * 16 - 15 * NG);
    unsigned base[4];
    {
        unsigned run = 0;
#pragma unroll
        for (int k = 0; k < 4; ++k) {
            const unsigned pad = ((tot[k] & (k < 3 ? 0x3FFFFFFFu : 0x3FFu)) + 0x00F03C0Fu) & 0x3F0FC3F0u;
            const unsigned incl = pad * 0x00100401u;
            base[k] = incl - pad + run * 0x00100401u;
            run += (incl >> 20) & 1023u;
        }
    }
    int qi, left;
    {
        const int jw = (wv * 11) >> 5, sh = (wv - 3 * jw) * 10;
        const unsigned bw = jw == 0 ? base[0] : (jw == 1 ? base[1] : (jw == 2 ? base[2] : base[3]));
        const unsigned tw = jw == 0 ? tot[0] : (jw == 1 ? tot[1] : (jw == 2 ? tot[2] : tot[3]));
        qi = (int)(((bw >> sh) & 1023u) >> 4);
        left = (int)((tw >> sh) & 1023u);
    }
        if (wv < 8) {
        unsigned pre0, pre1, pre2, pre3, ccnt;
        {
            unsigned ex[4];
#pragma unroll
            for (int k = 0; k < 4; ++k) ex[k] = inc[k] - ct[k] + base[k];
            ex[3] = (ex[3] & 1023u) | (ct[3] & (63u << 10));
            const int sl = (wv * 8 + (lane >> 3)) * 4;
            pre0 = (unsigned)__builtin_amdgcn_ds_bpermute(sl, (int)ex[0]);
            pre1 = (unsigned)__builtin_amdgcn_ds_bpermute(sl, (int)ex[1]);
            pre2 = (unsigned)__builtin_amdgcn_ds_bpermute(sl, (int)ex[2]);
            pre3 = (unsigned)__builtin_amdgcn_ds_bpermute(sl, (int)ex[3]);
            asm volatile("" : "+v"(pre0), "+v"(pre1), "+v"(pre2), "+v"(pre3));
            ccnt = slow ? 0u : (pre3 >> 10);
        }
#pragma unroll
        for (int k = 0; k < 4; ++k) {
            const unsigned e = ce[k];
            const bool j0 = (e >> 25) & 1u, j1 = (e >> 26) & 1u;
            const unsigned lo2 = j0 ? pre1 : pre0, hi2 = j0 ? pre3 : pre2;
            const unsigned dw = j1 ? hi2 : lo2;
            const unsigned slot = ((dw >> (e >> 27)) & 1023u) + ((e >> 20) & 31u);
            const unsigned r = slot & 15u;
            unsigned pos = (slot & ~15u) | ((r & 1u) << 3) | (r >> 1);
            pos = ((unsigned)((lane & 7) * 4 + k) < ccnt) ? pos : (unsigned)((MAXQ + 2) * 16 + lane);
            elist[pos] = (((e & 0x3FFFu) << 9) + 4096u - ((r & 6u) << 9)) | (sw2(r) << 4) | ((e >> 14) & 3u);
        }
    }
    unsigned rowa = gbuf_a + c16 * 512 + (g & 1) * 256 + ((sw2((unsigned)c16) ^ ((unsigned)(g >> 1) << 3)) << 4);
    const char* wap = smem + L_WA + lane * 16;
#define WAF(k) (*(const u32x4*)(wap + (k) * 1024))
#define LOGITS(LG) do { f32x4 l0 = {0.f, 0.f, 0.f, 0.f}, l1 = {0.f, 0.f, 0.f, 0.f}; \
        _Pragma("unroll") for (int ks = 0; ks < 8; ks += 2) { l0 = mfma16(wa[ks], xb[ks], l0); l1 = mfma16(wa[ks + 1], xb[ks + 1], l1); } \
        LG = l0 + l1; } while (0)
    u32x4 wa[8];
    u32x4 xb[8];
    LDS_BARRIER();
    if (left > 80) __builtin_amdgcn_s_setprio(3); else if (left > 64) __builtin_amdgcn_s_setprio(2); else __builtin_amdgcn_s_setprio(1);
    if (slow) left = 0;
    const int epos = ((c16 & 1) << 3) | (c16 >> 1);
    unsigned fe, ao[8];
#define PREP() do { \
        const u32x4 e0 = *(const u32x4*)(elist + qi * 16 + (lane >> 5) * 8), e1 = *(const u32x4*)(elist + qi * 16 + (lane >> 5) * 8 + 4); \
        fe = elist[qi * 16 + epos]; \
        _Pragma("unroll") for (int k = 0; k < 4; ++k) { ao[k] = (e0[k] & ~15u) ^ lch; ao[4 + k] = (e1[k] & ~15u) ^ lch; } } while (0)
#define ISSUE() do { \
        unsigned keep; \
        asm volatile("s_waitcnt lgkmcnt(0)\n\ts_mov_b32 %0, m0\n\ts_mov_b32 m0, %10\n\t" \
                     "s_cmp_lt_i32 %11, 15\n\ts_cbranch_scc1 .Lp%=\n\t" \
                     "buffer_load_dwordx4 %1, %9, 0 offen lds\n\tbuffer_load_dwordx4 %2, %9, 0 offen offset:1024 lds\n\t" \
                     "buffer_load_dwordx4 %3, %9, 0 offen offset:2048 lds\n\tbuffer_load_dwordx4 %4, %9, 0 offen offset:3072 lds\n\t" \
                     "s_add_u32 m0, m0, 0x1000\n\ts_nop 0\n\t" \
                     "buffer_load_dwordx4 %5, %9, 0 offen lds\n\tbuffer_load_dwordx4 %6, %9, 0 offen offset:1024 lds\n\t" \
                     "buffer_load_dwordx4 %7, %9, 0 offen offset:2048 lds\n\tbuffer_load_dwordx4 %8, %9, 0 offen offset:3072 lds\n\t" \
                     "s_branch .Lq%=\n.Lp%=:\n\t" \
                     "s_cmp_lt_i32 %11, 1\n\ts_cbranch_scc1 .Lq%=\n\tbuffer_load_dwordx4 %1, %9, 0 offen lds\n\t" \
                     "s_cmp_lt_i32 %11, 3\n\ts_cbranch_scc1 .Lq%=\n\tbuffer_load_dwordx4 %2, %9, 0 offen offset:1024 lds\n\t" \
                     "s_cmp_lt_i32 %11, 5\n\ts_cbranch_scc1 .Lq%=\n\tbuffer_load_dwordx4 %3, %9, 0 offen offset:2048 lds\n\t" \
                     "s_cmp_lt_i32 %11, 7\n\ts_cbranch_scc1 .Lq%=\n\tbuffer_load_dwordx4 %4, %9, 0 offen offset:3072 lds\n\t" \
                     "s_add_u32 m0, m0, 0x1000\n\t" \
                     "s_cmp_lt_i32 %11, 9\n\ts_cbranch_scc1 .Lq%=\n\tbuffer_load_dwordx4 %5, %9, 0 offen lds\n\t" \
                     "s_cmp_lt_i32 %11, 11\n\ts_cbranch_scc1 .Lq%=\n\tbuffer_load_dwordx4 %6, %9, 0 offen offset:1024 lds\n\t" \
                     "s_cmp_lt_i32 %11, 13\n\ts_cbranch_scc1 .Lq%=\n\tbuffer_load_dwordx4 %7, %9, 0 offen offset:2048 lds\n" \
                     ".Lq%=:\n\ts_mov_b32 m0, %0" \
                     : "=&s"(keep) \
                     : "v"(ao[0]), "v"(ao[1]), "v"(ao[2]), "v"(ao[3]), "v"(ao[4]), "v"(ao[5]), "v"(ao[6]), "v"(ao[7]), \
                       "s"(xr), "s"(gbuf_a), "s"(left) : "scc", "memory"); } while (0)
    PREP();
    asm volatile("s_cmp_lt_i32 %0, 8\n\ts_cbranch_scc1 .Lwa%=\n\ts_waitcnt vmcnt(%1)\n\ts_branch .Lwb%=\n"
                 ".Lwa%=:\n\ts_waitcnt vmcnt(0)\n.Lwb%=:" :: "s"(wv), "i"(4 * NLF) : "scc", "memory");
    ISSUE();
    float sdq;
    {
        const unsigned oa = own_a + (c16 & 3) * 512 + (g & 1) * 256 + ((sw2((unsigned)(c16 & 3)) ^ ((unsigned)(g >> 1) << 3)) << 4);
#pragma unroll
        for (int ks = 0; ks < 8; ++ks) xb[ks] = *(const LAS u32x4*)(uintptr_t)(oa ^ ((unsigned)ks << 4));
#pragma unroll
        for (int k = 0; k < 8; ++k) wa[k] = WAF(k);
        f32x4 lg; LOGITS(lg); sdq = lg[2] + lg[3];
    }
    if (!slow) {
        const s16x4 ones2 = {0x3F80, 0x3F80, 0x3F80, 0x3F80};
        char* pscr = smem + L_P + wv * 2048;
        f32x4 acc[16], accd = {0.f, 0.f, 0.f, 0.f};
#pragma unroll
        for (int i = 0; i < 16; ++i) acc[i] = f32x4{0.f, 0.f, 0.f, 0.f};
        const unsigned trow = 4u * g + ((unsigned)c16 >> 2), tp = (unsigned)c16 & 3u;
        unsigned tra = gbuf_a + trow * 512 + (tp & 1u) * 8 + ((sw2(trow) ^ (tp >> 1)) << 4);
        asm volatile("" ::: "memory");
        *(u32x2*)(pscr + lane * 8) = u32x2{0u, 0u};
#pragma unroll 1
        while (left > 0) {
            left -= 16; qi += 1;
            const unsigned fc = fe;
            const float sdl = __builtin_bit_cast(float, __builtin_amdgcn_ds_bpermute((int)((g * 16 + (fc & 3u)) * 4), __builtin_bit_cast(int, sdq)));
            asm volatile("s_waitcnt vmcnt(0)" ::: "memory");
            asm volatile("" : "+v"(rowa));
            f32x4 l0 = {0.f, 0.f, 0.f, 0.f}, l1 = {0.f, 0.f, 0.f, 0.f};
#define XR(ks) (*(const LAS u32x4*)(uintptr_t)(rowa ^ ((unsigned)(ks) << 4)))
            {
                const u32x4 x0 = XR(0), x1 = XR(1), x2 = XR(2), x3 = XR(3), x4 = XR(4), x5 = XR(5);
                __builtin_amdgcn_sched_barrier(0);
                l0 = mfma16(wa[0], x0, l0); l1 = mfma16(wa[1], x1, l1);
                l0 = mfma16(wa[2], x2, l0); l1 = mfma16(wa[3], x3, l1);
                __builtin_amdgcn_sched_barrier(0);
                const u32x4 x6 = XR(6), x7 = XR(7);
                __builtin_amdgcn_sched_barrier(0);
                l0 = mfma16(wa[4], x4, l0); l1 = mfma16(wa[5], x5, l1);
                l0 = mfma16(wa[6], x6, l0); l1 = mfma16(wa[7], x7, l1);
                __builtin_amdgcn_sched_barrier(0);
            }
            s16x4 tf[16];
            asm volatile("" : "+v"(tra));
#pragma unroll
            for (int t = 0; t < 8; ++t) {
                const unsigned ta = tra ^ ((unsigned)t << 5);
                tf[t] = __builtin_amdgcn_ds_read_tr16_b64_v4i16((LAS s16x4*)(uintptr_t)ta);
                tf[t + 8] = __builtin_amdgcn_ds_read_tr16_b64_v4i16((LAS s16x4*)(uintptr_t)(ta + 256));
            }
            __builtin_amdgcn_sched_barrier(0);
            float e = ((l0[0] + l1[0]) + (l0[1] + l1[1])) + sdl;
            e = e > 0.f ? e : 0.2f * e;
            float p = __expf(e);
            p = fc != OOB ? p : 0.f;
            __builtin_amdgcn_sched_barrier(0);
            PREP();
            *(__bf16*)(pscr + ((c16 >> 2) * 16 + (fc & 3u) * 4 + g) * 8 + (c16 & 3) * 2) = (__bf16)p;
            asm volatile("" ::: "memory");
            const u32x2 pf = *(const u32x2*)(pscr + lane * 8);
            *(u32x2*)(pscr + lane * 8) = u32x2{0u, 0u};
            ISSUE();
#pragma unroll
            for (int t = 0; t < 16; ++t) acc[t] = mfma16h(tf[t], pf, acc[t]);
            accd = mfma16h(ones2, pf, accd);
        }
        {
            const float sc = 0.25f * __builtin_amdgcn_rcpf(accd[0] + 1e-9f);
            const int node = wv * 4 + (c16 >> 2), h = c16 & 3;
#pragma unroll
            for (int u = 0; u < 8; ++u) {
                const f32x4 a = acc[2 * u] * sc, b2 = acc[2 * u + 1] * sc;
                u32x4 o;
                o[0] = pack_bf16(a[0], a[1]); o[1] = pack_bf16(a[2], a[3]);
                o[2] = pack_bf16(b2[0], b2[1]); o[3] = pack_bf16(b2[2], b2[3]);
                *(u32x4*)(smem + L_Z + zaddr(node, h * 32 + 4 * u + g)) = o;
            }
        }
    } else {
        float wsv[4][4];
        {
            const int d = 4 * lane, cd = d >> 3;
            const int ks = cd & 7, kq = ((cd >> 4) & 1) | (((cd >> 3) & 1) << 1);
#pragma unroll
            for (int h = 0; h < 4; ++h) {
                const u32x2 hi = *(const u32x2*)(ws + WS_WA + ks * 1024 + (kq * 16 + 4 * h) * 16 + (d & 7) * 2);
                const u32x2 lo = *(const u32x2*)(ws + WS_WA + ks * 1024 + (kq * 16 + 4 * h + 1) * 16 + (d & 7) * 2);
                wsv[h][0] = __uint_as_float(hi[0] << 16) + __uint_as_float(lo[0] << 16);
                wsv[h][1] = __uint_as_float(hi[0] & 0xFFFF0000u) + __uint_as_float(lo[0] & 0xFFFF0000u);
                wsv[h][2] = __uint_as_float(hi[1] << 16) + __uint_as_float(lo[1] << 16);
                wsv[h][3] = __uint_as_float(hi[1] & 0xFFFF0000u) + __uint_as_float(lo[1] & 0xFFFF0000u);
            }
        }
#pragma unroll 1
        for (int nn = 0; nn < 4; ++nn) {
            const int nl = wv * 4 + nn;
            const int node = n0 + nl;
            float acc[4][4], den[4], sdn[4];
#pragma unroll
            for (int h = 0; h < 4; ++h) { den[h] = 0.f; sdn[h] = __shfl(sdq, h * 16 + nn, 64);
#pragma unroll
                for (int i = 0; i < 4; ++i) acc[h][i] = 0.f; }
#pragma unroll 1
            for (int e0 = 0; e0 < NE; e0 += 64) {
                const int e = e0 + lane;
                const int dv = dst[e];
                const int sv = src[e];
                unsigned long long mask = __ballot(dv == node);
                while (mask) {
                    const int bit = __builtin_ctzll(mask);
                    mask &= mask - 1;
                    const int s = __shfl(sv, bit, 64);
                    const uint2 xv = *(const uint2*)(x + (size_t)s * DD + lane * 4);
                    const float xf[4] = {__uint_as_float(xv.x << 16), __uint_as_float(xv.x & 0xFFFF0000u),
                                         __uint_as_float(xv.y << 16), __uint_as_float(xv.y & 0xFFFF0000u)};
#pragma unroll
                    for (int h = 0; h < 4; ++h) {
                        float dsum = (xf[0] * wsv[h][0] + xf[1] * wsv[h][1]) + (xf[2] * wsv[h][2] + xf[3] * wsv[h][3]);
#pragma unroll
                        for (int o = 32; o > 0; o >>= 1) dsum += __shfl_xor(dsum, o, 64);
                        float ev = dsum + sdn[h];
                        ev = ev > 0.f ? ev : 0.2f * ev;
                        const float p = bf16_round(__expf(ev));
                        den[h] += p;
#pragma unroll
                        for (int i = 0; i < 4; ++i) acc[h][i] += p * xf[i];
                    }
                }
            }
#pragma unroll
            for (int h = 0; h < 4; ++h) {
                const float sc = 0.25f / (den[h] + 1e-9f);
                uint2 o;
                o.x = pack_bf16(acc[h][0] * sc, acc[h][1] * sc);
                o.y = pack_bf16(acc[h][2] * sc, acc[h][3] * sc);
                *(uint2*)(smem + L_Z + zaddr(nl, h * 32 + 4 * (lane >> 3) + (lane & 3)) + ((lane >> 2) & 1) * 8) = o;
            }
        }
    }
    asm volatile("s_waitcnt vmcnt(0)" ::: "memory");
    __builtin_amdgcn_s_setprio(0);
        if (wv >= 8) {
        LDS_BARRIER();
                return;
    }
    const __amdgpu_buffer_rsrc_t wtr = __builtin_amdgcn_make_buffer_rsrc((void*)(ws + WS_WT), 0, 524288, 0x00020000);
    const int wto = wv * 65536 + lane * 16;
#define WLD(i) __builtin_amdgcn_raw_buffer_load_b128(wtr, wto + ((i) & 3) * 1024, ((i) >> 2) * 4096, 0)
    u32x4 wr[WIN];
#pragma unroll
    for (int i = 0; i < WIN; ++i) wr[i] = WLD(NLF + i);
    __builtin_amdgcn_sched_barrier(0);
    LDS_BARRIER();
    {
        f32x4 acc[2][3];
#pragma unroll
        for (int t = 0; t < 2; ++t)
#pragma unroll
            for (int nt = 0; nt < 3; ++nt) acc[t][nt] = f32x4{0.f, 0.f, 0.f, 0.f};
        u32x4 zf[2][3];
#pragma unroll
        for (int nt = 0; nt < 3; ++nt) zf[0][nt] = *(const u32x4*)(smem + L_Z + zaddr(nt * 16 + c16, g));
#pragma unroll
        for (int ks = 0; ks < 32; ++ks) {
            if (ks + 1 < 32) {
#pragma unroll
                for (int nt = 0; nt < 3; ++nt)
                    zf[(ks + 1) & 1][nt] = *(const u32x4*)(smem + L_Z + zaddr(nt * 16 + c16, 4 * (ks + 1) + g));
            }
            u32x4 w0, w1;
            if (2 * ks < NLF) {
                w0 = *(const u32x4*)(smem + L_W + (wv * NLF + 2 * ks) * 1024 + lane * 16);
                w1 = *(const u32x4*)(smem + L_W + (wv * NLF + 2 * ks + 1) * 1024 + lane * 16);
            } else {
                w0 = wr[(ks * 2 - NLF) % WIN]; w1 = wr[(ks * 2 + 1 - NLF) % WIN];
            }
#pragma unroll
            for (int nt = 0; nt < 3; ++nt) {
                acc[0][nt] = mfma16(w0, zf[ks & 1][nt], acc[0][nt]);
                acc[1][nt] = mfma16(w1, zf[ks & 1][nt], acc[1][nt]);
            }
            if (2 * ks >= NLF && ks * 2 + WIN < 64) {
                wr[(ks * 2 - NLF) % WIN] = WLD(ks * 2 + WIN);
                wr[(ks * 2 + 1 - NLF) % WIN] = WLD(ks * 2 + 1 + WIN);
            }
            __builtin_amdgcn_sched_barrier(0);
        }
                const __amdgpu_buffer_rsrc_t outr = __builtin_amdgcn_make_buffer_rsrc((void*)out, 0, NN * DD * 2, 0x00020000);
#pragma unroll
        for (int nt = 0; nt < 3; ++nt) {
            const int nl = nt * 16 + c16;
            if (nl < T) {
                u32x4 o;
                o[0] = pack_bf16(acc[0][nt][0], acc[0][nt][1]);
                o[1] = pack_bf16(acc[0][nt][2], acc[0][nt][3]);
                o[2] = pack_bf16(acc[1][nt][0], acc[1][nt][1]);
                o[3] = pack_bf16(acc[1][nt][2], acc[1][nt][3]);
                __builtin_amdgcn_raw_buffer_store_b128(o, outr, ((n0 + nl) * DD + 32 * wv + 8 * g) * 2, 0, 16);
            }
        }
    }
        asm volatile("gat_k2_tail_label:");
}

extern "C" void kernel_launch(void* const* d_in, const int* in_sizes, int n_in, void* d_out, int out_size,
                              void* d_ws, size_t ws_size, hipStream_t stream) {
    const unsigned short* x = (const unsigned short*)d_in[0];
    const int* src = (const int*)d_in[1];
    const int* dst = (const int*)d_in[2];
    const unsigned short* w = (const unsigned short*)d_in[3];
    const unsigned short* a_src = (const unsigned short*)d_in[4];
    const unsigned short* a_dst = (const unsigned short*)d_in[5];
    (void)hipFuncSetAttribute((const void*)gat_k2, hipFuncAttributeMaxDynamicSharedMemorySize, L_TOTAL);
    gat_k1<<<256, 512, K1L_TOTAL, stream>>>(x, src, dst, w, a_src, a_dst, (char*)d_ws);
    gat_k2<<<NT, 640, L_TOTAL, stream>>>(x, src, dst, (const char*)d_ws, (unsigned short*)d_out);
}
```
